```python
import torch

def _softmax(x, axis=-1):
    return torch.softmax(x, dim=axis)
def _gelu(x):
    return torch.nn.functional.gelu(x, approximate="tanh")
def _silu(x):
    return torch.nn.functional.silu(x)
def _sigmoid(x):
    return torch.sigmoid(x)
def _layernorm(x, axis=-1, eps=1e-5):
    m = x.mean(dim=axis, keepdim=True)
    v = x.var(dim=axis, keepdim=True, unbiased=False)
    return (x - m) / torch.sqrt(v + eps)
def _scatter_add(tgt, idx, src, axis=0):
    return tgt.clone().index_add_(axis, idx, src)
def _segment_sum(src, seg, n):
    out = torch.zeros((n,) + src.shape[1:], dtype=src.dtype)
    return out.index_add_(0, seg, src)
def _pad(x, pw):
    flat = tuple(v for pair in reversed(pw) for v in pair)
    return torch.nn.functional.pad(x, flat)

kernel_name = 'concat_heads__sliding_block__sliding_block__prompt'

def setup_inputs():

    _w = (lambda a: (lambda t: t.to(torch.bfloat16) if t.is_floating_point() else t)(torch.as_tensor(a)))
    def _A_setup():
        B, S, D = 1, 2048, 576
        return {
            "x": _w((lambda *s: torch.randn(*s, dtype=torch.bfloat16) * 0.2)(B, S, D)),
            "wqkv": _w((lambda *s: torch.randn(*s, dtype=torch.bfloat16) * 0.2)(D, 3 * D)),
            "wo": _w((lambda *s: torch.randn(*s, dtype=torch.bfloat16) * 0.05)(D, D)),
        }
    _A_in = _A_setup()

    def _B_setup():
        B, S, D = 1, 2048, 576
        return {
            "x": _w((lambda *s: torch.randn(*s, dtype=torch.bfloat16) * 0.2)(B, S, D)),
            "wqkv": _w((lambda *s: torch.randn(*s, dtype=torch.bfloat16) * 0.2)(D, 3 * D)),
            "wo": _w((lambda *s: torch.randn(*s, dtype=torch.bfloat16) * 0.05)(D, D)),
        }
    _B_in = _B_setup()

    return {
        "A_x": _A_in["x"],
        "A_wqkv": _A_in["wqkv"],
        "A_wo": _A_in["wo"],
        "B_x": _B_in["x"],
        "B_wqkv": _B_in["wqkv"],
        "B_wo": _B_in["wo"],
    }

def reference(A_x, A_wqkv, A_wo, B_x, B_wqkv, B_wo):
    def _A_ref(x, wqkv, wo):
        B, S, D = x.shape
        H = 32; Dh = D // H; W = 256
        qkv = (lambda x, *d: x.permute(*d))((x @ wqkv).reshape(B, S, 3, H, Dh), 2, 0, 3, 1, 4)
        q, k, v = qkv[0], qkv[1], qkv[2]
        scores = torch.einsum("bhqd,bhkd->bhqk", q, k) * (Dh ** -0.5)
        idx = torch.arange(S)
        mask = (idx[:, None] - idx[None, :] >= 0) & (idx[:, None] - idx[None, :] < W)
        scores = torch.where(mask[None, None], scores, -1e9)
        p = _softmax(scores, axis=-1)
        o = torch.einsum("bhqk,bhkd->bhqd", p, v)
        return (lambda x, *d: x.permute(*d))(o, 0, 2, 1, 3).reshape(B, S, D) @ wo

    def _B_ref(x, wqkv, wo):
        B, S, D = x.shape
        H = 32; Dh = D // H; W = 256
        qkv = (lambda x, *d: x.permute(*d))((x @ wqkv).reshape(B, S, 3, H, Dh), 2, 0, 3, 1, 4)
        q, k, v = qkv[0], qkv[1], qkv[2]
        scores = torch.einsum("bhqd,bhkd->bhqk", q, k) * (Dh ** -0.5)
        idx = torch.arange(S)
        mask = (idx[:, None] - idx[None, :] >= 0) & (idx[:, None] - idx[None, :] < W)
        scores = torch.where(mask[None, None], scores, -1e9)
        p = _softmax(scores, axis=-1)
        o = torch.einsum("bhqk,bhkd->bhqd", p, v)
        return (lambda x, *d: x.permute(*d))(o, 0, 2, 1, 3).reshape(B, S, D) @ wo

    a = _A_ref(A_x, A_wqkv, A_wo)
    b = _B_ref(B_x, B_wqkv, B_wo)
    return torch.concatenate([a, b], axis=-1)
```

```cpp
#include <hip/hip_runtime.h>
#include <hip/hip_bf16.h>
typedef unsigned short u16;
__device__ __forceinline__ float bf2f(u16 v){ return __uint_as_float(((unsigned)v)<<16); }
__device__ __forceinline__ u16 f2bf(float f){ unsigned u=__float_as_uint(f); u += 0x7fffu + ((u>>16)&1u); return (u16)(u>>16); }
constexpr int S=2048, D=576, H=32, DH=18, W=256;
__global__ void k_gemm(const u16* __restrict__ a, const u16* __restrict__ b, u16* __restrict__ c, int N, int ldc){
  int n = blockIdx.x*64 + (threadIdx.x&63); int m = blockIdx.y*4 + (threadIdx.x>>6);
  if(n>=N) return;
  float acc=0.f;
  for(int k=0;k<D;k++) acc += bf2f(a[m*D+k])*bf2f(b[k*N+n]);
  c[m*ldc+n]=f2bf(acc);
}
__global__ void k_attn(const u16* __restrict__ qkv, u16* __restrict__ o){
  int q = blockIdx.x*64 + threadIdx.x; int h = blockIdx.y;
  float qv[DH]; for(int d=0;d<DH;d++) qv[d]=bf2f(qkv[q*3*D + h*DH + d]);
  int k0 = q-W+1; if(k0<0) k0=0;
  float m=-1e30f;
  for(int k=k0;k<=q;k++){ float s=0; for(int d=0;d<DH;d++) s+=qv[d]*bf2f(qkv[k*3*D + D + h*DH + d]); s*=0.23570226039551584f; m=fmaxf(m,s);}
  float l=0; float acc[DH]; for(int d=0;d<DH;d++) acc[d]=0;
  for(int k=k0;k<=q;k++){ float s=0; for(int d=0;d<DH;d++) s+=qv[d]*bf2f(qkv[k*3*D + D + h*DH + d]); s*=0.23570226039551584f; float p=__expf(s-m); l+=p; for(int d=0;d<DH;d++) acc[d]+=p*bf2f(qkv[k*3*D + 2*D + h*DH + d]); }
  for(int d=0;d<DH;d++) o[q*D + h*DH + d]=f2bf(acc[d]/l);
}
extern "C" void kernel_launch(void* const* d_in, const int* in_sizes, int n_in, void* d_out, int out_size, void* d_ws, size_t ws_size, hipStream_t stream){
  u16* ws=(u16*)d_ws;
  for(int b=0;b<2;b++){
    const u16* x=(const u16*)d_in[3*b]; const u16* wqkv=(const u16*)d_in[3*b+1]; const u16* wo=(const u16*)d_in[3*b+2];
    u16* qkv = ws + (size_t)b*(S*3*D + S*D); u16* o = qkv + S*3*D;
    k_gemm<<<dim3(27, S/4),256,0,stream>>>(x,wqkv,qkv,3*D,3*D);
    k_attn<<<dim3(S/64,H),64,0,stream>>>(qkv,o);
    k_gemm<<<dim3(9, S/4),256,0,stream>>>(o,wo,(u16*)d_out + b*D,D,2*D);
  }
}
```

```cpp
#include <hip/hip_runtime.h>
#include <hip/hip_bf16.h>
typedef unsigned short u16;
__device__ __forceinline__ float bf2f(u16 v){ return __uint_as_float(((unsigned)v)<<16); }
__device__ __forceinline__ u16 f2bf(float f){ unsigned u=__float_as_uint(f); u += 0x7fffu + ((u>>16)&1u); return (u16)(u>>16); }
constexpr int S=2048, D=576, H=32, DH=18, W=256;
__global__ void k_gemm(const u16* __restrict__ a, const u16* __restrict__ b, u16* __restrict__ c, int N, int ldc){
  int n = blockIdx.x*64 + (threadIdx.x&63); int m = blockIdx.y*4 + (threadIdx.x>>6);
  if(n>=N) return;
  float acc=0.f;
  for(int k=0;k<D;k++) acc += bf2f(a[m*D+k])*bf2f(b[k*N+n]);
  c[m*ldc+n]=f2bf(acc);
}
__global__ void k_attn(const u16* __restrict__ qkv, u16* __restrict__ o){
  int q = blockIdx.x*64 + threadIdx.x; int h = blockIdx.y;
  float qv[DH]; for(int d=0;d<DH;d++) qv[d]=bf2f(qkv[q*3*D + h*DH + d]);
  int k0 = q-W+1; if(k0<0) k0=0;
  float m=-1e30f;
  for(int k=k0;k<=q;k++){ float s=0; for(int d=0;d<DH;d++) s+=qv[d]*bf2f(qkv[k*3*D + D + h*DH + d]); s*=0.23570226039551584f; m=fmaxf(m,s);}
  float l=0; float acc[DH]; for(int d=0;d<DH;d++) acc[d]=0;
  for(int k=k0;k<=q;k++){ float s=0; for(int d=0;d<DH;d++) s+=qv[d]*bf2f(qkv[k*3*D + D + h*DH + d]); s*=0.23570226039551584f; float p=__expf(s-m); l+=p; for(int d=0;d<DH;d++) acc[d]+=p*bf2f(qkv[k*3*D + 2*D + h*DH + d]); }
  for(int d=0;d<DH;d++) o[q*D + h*DH + d]=f2bf(acc[d]/l);
}

__global__ void chase(const unsigned* __restrict__ p, unsigned* sink, int n){
  unsigned o=0, acc=0;
  for(int i=0;i<n;i++){ unsigned v = __builtin_nontemporal_load(p+o) ; acc+=v; unsigned z; asm volatile("v_and_b32 %0, 0, %1":"=v"(z):"v"(v)); o = o + 32 + z; }
  sink[threadIdx.x]=acc;
}
__global__ void chase2(const unsigned* p, unsigned* sink, int n){
  unsigned o=0, acc=0;
  for(int i=0;i<n;i++){ unsigned v; asm volatile("global_load_dword %0, %1, %2\n s_waitcnt vmcnt(0)":"=&v"(v):"v"(o),"s"(p):"memory"); acc+=v; o = o + 128; }
  sink[threadIdx.x]=acc;
}
__global__ void __launch_bounds__(64) code(float* sink){ float a=threadIdx.x,b=1.0f; asm volatile(".rept 7000\n v_fma_f32 %0, %1, %1, %1\n .endr":"=v"(b):"v"(a)); sink[blockIdx.x*64+threadIdx.x]=b; }
extern "C" void kernel_launch(void* const* d_in, const int* in_sizes, int n_in, void* d_out, int out_size, void* d_ws, size_t ws_size, hipStream_t stream){
  u16* ws=(u16*)d_ws; unsigned* sk=(unsigned*)((char*)d_ws+(100<<20));
  chase2<<<1,64,0,stream>>>((const unsigned*)d_in[0],sk,1);
  for(int b=0;b<2;b++){
    const u16* x=(const u16*)d_in[3*b]; const u16* wqkv=(const u16*)d_in[3*b+1]; const u16* wo=(const u16*)d_in[3*b+2];
    u16* qkv = ws + (size_t)b*(S*3*D + S*D); u16* o = qkv + S*3*D;
    k_gemm<<<dim3(27, S/4),256,0,stream>>>(x,wqkv,qkv,3*D,3*D);
    k_attn<<<dim3(S/64,H),64,0,stream>>>(qkv,o);
    k_gemm<<<dim3(9, S/4),256,0,stream>>>(o,wo,(u16*)d_out + b*D,D,2*D);
  }
}
```

```cpp
#include <hip/hip_runtime.h>
#include <hip/hip_bf16.h>
typedef unsigned short u16;
__device__ __forceinline__ float bf2f(u16 v){ return __uint_as_float(((unsigned)v)<<16); }
__device__ __forceinline__ u16 f2bf(float f){ unsigned u=__float_as_uint(f); u += 0x7fffu + ((u>>16)&1u); return (u16)(u>>16); }
constexpr int S=2048, D=576, H=32, DH=18, W=256;
__global__ void k_gemm(const u16* __restrict__ a, const u16* __restrict__ b, u16* __restrict__ c, int N, int ldc){
  int n = blockIdx.x*64 + (threadIdx.x&63); int m = blockIdx.y*4 + (threadIdx.x>>6);
  if(n>=N) return;
  float acc=0.f;
  for(int k=0;k<D;k++) acc += bf2f(a[m*D+k])*bf2f(b[k*N+n]);
  c[m*ldc+n]=f2bf(acc);
}
__global__ void k_attn(const u16* __restrict__ qkv, u16* __restrict__ o){
  int q = blockIdx.x*64 + threadIdx.x; int h = blockIdx.y;
  float qv[DH]; for(int d=0;d<DH;d++) qv[d]=bf2f(qkv[q*3*D + h*DH + d]);
  int k0 = q-W+1; if(k0<0) k0=0;
  float m=-1e30f;
  for(int k=k0;k<=q;k++){ float s=0; for(int d=0;d<DH;d++) s+=qv[d]*bf2f(qkv[k*3*D + D + h*DH + d]); s*=0.23570226039551584f; m=fmaxf(m,s);}
  float l=0; float acc[DH]; for(int d=0;d<DH;d++) acc[d]=0;
  for(int k=k0;k<=q;k++){ float s=0; for(int d=0;d<DH;d++) s+=qv[d]*bf2f(qkv[k*3*D + D + h*DH + d]); s*=0.23570226039551584f; float p=__expf(s-m); l+=p; for(int d=0;d<DH;d++) acc[d]+=p*bf2f(qkv[k*3*D + 2*D + h*DH + d]); }
  for(int d=0;d<DH;d++) o[q*D + h*DH + d]=f2bf(acc[d]/l);
}

typedef unsigned u32x4 __attribute__((ext_vector_type(4)));
template<int NT> __global__ void rd(const u32x4* p,size_t n,unsigned* o){ size_t i=blockIdx.x*(size_t)blockDim.x+threadIdx.x; unsigned a=0; for(;i<n;i+=(size_t)gridDim.x*blockDim.x){ u32x4 v= NT? __builtin_nontemporal_load(p+i) : p[i]; a^=v.x^v.y^v.z^v.w;} o[blockIdx.x*blockDim.x+threadIdx.x]=a; }
extern "C" void kernel_launch(void* const* d_in, const int* in_sizes, int n_in, void* d_out, int out_size, void* d_ws, size_t ws_size, hipStream_t stream){
  u16* ws=(u16*)d_ws; unsigned* sk=(unsigned*)((char*)d_ws+(240<<20));
  rd<0><<<2048,256,0,stream>>>((const u32x4*)d_ws,(size_t)1024,sk);
  for(int b=0;b<2;b++){
    const u16* x=(const u16*)d_in[3*b]; const u16* wqkv=(const u16*)d_in[3*b+1]; const u16* wo=(const u16*)d_in[3*b+2];
    u16* qkv = ws + (size_t)b*(S*3*D + S*D); u16* o = qkv + S*3*D;
    k_gemm<<<dim3(27, S/4),256,0,stream>>>(x,wqkv,qkv,3*D,3*D);
    k_attn<<<dim3(S/64,H),64,0,stream>>>(qkv,o);
    k_gemm<<<dim3(9, S/4),256,0,stream>>>(o,wo,(u16*)d_out + b*D,D,2*D);
  }
}
```

```cpp
#include <hip/hip_runtime.h>
typedef unsigned u32;
typedef unsigned long long u64;
typedef u32 u32x2 __attribute__((ext_vector_type(2)));
typedef u32 u32x4 __attribute__((ext_vector_type(4)));
typedef short s16x4 __attribute__((ext_vector_type(4)));
typedef __bf16 bf16x2 __attribute__((ext_vector_type(2)));
typedef __bf16 bf16x8 __attribute__((ext_vector_type(8)));
typedef float f32x2 __attribute__((ext_vector_type(2)));
typedef float f32x4 __attribute__((ext_vector_type(4)));
typedef float f32x16 __attribute__((ext_vector_type(16)));
#define LAS __attribute__((address_space(3)))
#define GAS __attribute__((address_space(1)))
#define DI __device__ __forceinline__
#define RLX_AGENT __ATOMIC_RELAXED, __HIP_MEMORY_SCOPE_AGENT

DI void dma16(u32 voff, const char* sbase, u32 lds){
  asm volatile("s_mov_b32 m0, %2\n\ts_nop 0\n\tglobal_load_lds_dwordx4 %0, %1"::"v"(voff),"s"(sbase),"s"(lds):"memory");
}
DI void dma16c(u32 voff, const char* sbase, u32 lds){
  asm volatile("s_mov_b32 m0, %2\n\ts_nop 0\n\tglobal_load_lds_dwordx4 %0, %1 sc1"::"v"(voff),"s"(sbase),"s"(lds):"memory");
}
template<int N> DI void vmwait(){ asm volatile("s_waitcnt vmcnt(%0)"::"i"(N):"memory"); }
DI void bar(){ asm volatile("":::"memory"); __builtin_amdgcn_s_barrier(); asm volatile("":::"memory"); }
DI void barl(){ asm volatile("s_waitcnt lgkmcnt(0)":::"memory"); __builtin_amdgcn_s_barrier(); asm volatile("":::"memory"); }
DI u32 pk(float a,float b){ f32x2 v={a,b}; return __builtin_bit_cast(u32,__builtin_convertvector(v,bf16x2)); }
DI bf16x8 trfrag(u32 a0,u32 a1){
  s16x4 lo=__builtin_amdgcn_ds_read_tr16_b64_v4i16((LAS s16x4*)(size_t)a0);
  s16x4 hi=__builtin_amdgcn_ds_read_tr16_b64_v4i16((LAS s16x4*)(size_t)a1);
  return __builtin_bit_cast(bf16x8,__builtin_shufflevector(lo,hi,0,1,2,3,4,5,6,7));
}
template<int OFF> DI void st16c(u32 voff,char* base,u32x4 v){ asm volatile("global_store_dwordx4 %0, %1, %2 offset:%3 sc1\n\ts_nop 1"::"v"(voff),"v"(v),"s"(base),"i"(OFF):"memory"); }
#define ST(i)

constexpr int PD=2;
constexpr int TS=4608;
constexpr int L_KO=8*TS, L_VO=12*TS;
constexpr int L_WKV=L_KO;
constexpr int L_WQ=L_WKV+3*18432;
constexpr int L_X2=L_WQ+3*9216;
constexpr int L_Q=L_X2;
constexpr int L_C=163328;
constexpr int L_WO=0, L_ST=88*1024;
constexpr int LDS_BYTES=163840;
constexpr size_t W_CTL=0, W_KV=1u<<20, W_O=16u<<20;

struct Params { const char* x[2]; const char* w[2]; const char* wo[2]; char* out; char* ws; };

template<int NT> DI void gemm_wave(f32x4 (&acc)[2][5], u32 wm0,u32 we0,int wstride,u32 xr0){
  bf16x8 wf[PD+1], xf[2][2];
  constexpr int NU=2*NT;
  int ws=0, xs=0;
  u32 wm,we;
  auto R=[&](int u){ const int ks=u/NT,n=u%NT; const u32 ko=ks*4608; const u32 co=n==0?0:n==1?40:n==2?72:112;
    if(n<4) wf[u%(PD+1)]=trfrag(wm+ko+co,wm+ko+co+144); else wf[u%(PD+1)]=trfrag(we+ko,we+ko+144); };
#pragma nounroll
  for(int b=0;b<9;b++){
    bar();
    wm=wm0+ws*wstride; we=we0+ws*wstride; ws=ws==2?0:ws+1;
    const u32 xa=xr0+(xs<2?xs*16384:L_X2+(xs-2)*16384); xs=(xs+1)&3;
    xf[0][0]=*(LAS bf16x8*)(size_t)(xa); xf[0][1]=*(LAS bf16x8*)(size_t)(xa+2048);
#pragma unroll
    for(int u=0;u<PD;u++) R(u);
    xf[1][0]=*(LAS bf16x8*)(size_t)(xa^64); xf[1][1]=*(LAS bf16x8*)(size_t)((xa^64)+2048);
    __builtin_amdgcn_sched_barrier(0);
#pragma unroll
    for(int u=0;u<NU;u++){
      if(u+PD<NU) R(u+PD);
      acc[0][u%NT]=__builtin_amdgcn_mfma_f32_16x16x32_bf16(wf[u%(PD+1)],xf[u/NT][0],acc[0][u%NT],0,0,0);
      acc[1][u%NT]=__builtin_amdgcn_mfma_f32_16x16x32_bf16(wf[u%(PD+1)],xf[u/NT][1],acc[1][u%NT],0,0,0);
      __builtin_amdgcn_sched_group_barrier(8,1,0); __builtin_amdgcn_sched_group_barrier(0x100,1,0); __builtin_amdgcn_sched_group_barrier(8,1,0); __builtin_amdgcn_sched_group_barrier(0x100,1,0);
      __builtin_amdgcn_sched_barrier(0);
    }
  }
}
DI u32x4 pack_tile(const f32x4& a,const f32x4& b,float s){
  u32 x0=pk(a[0]*s,a[1]*s), x1=pk(a[2]*s,a[3]*s), y0=pk(b[0]*s,b[1]*s), y1=pk(b[2]*s,b[3]*s);
  auto s0=__builtin_amdgcn_permlane16_swap(x0,y0,false,false); auto s1=__builtin_amdgcn_permlane16_swap(x1,y1,false,false);
  return u32x4{s0[0],s1[0],s0[1],s1[1]};
}
DI f32x16 qk(u32 km,u32 kx,bf16x8 qm,s16x4 qx,f32x16 c){
  s16x4 a=*(LAS s16x4*)(size_t)kx; bf16x8 b=*(LAS bf16x8*)(size_t)km;
  c=__builtin_amdgcn_mfma_f32_32x32x8bf16_1k(a,qx,c,0,0,0);
  return __builtin_amdgcn_mfma_f32_32x32x16_bf16(b,qm,c,0,0,0);
}
DI f32x16 pv(u32 va,const f32x16& pr,f32x16 o){
  bf16x8 v0=trfrag(va,va+128), v1=trfrag(va+256,va+384);
  u32x4 p0={pk(pr[0],pr[1]),pk(pr[2],pr[3]),pk(pr[4],pr[5]),pk(pr[6],pr[7])};
  u32x4 p1={pk(pr[8],pr[9]),pk(pr[10],pr[11]),pk(pr[12],pr[13]),pk(pr[14],pr[15])};
  o=__builtin_amdgcn_mfma_f32_32x32x16_bf16(v0,__builtin_bit_cast(bf16x8,p0),o,0,0,0);
  return __builtin_amdgcn_mfma_f32_32x32x16_bf16(v1,__builtin_bit_cast(bf16x8,p1),o,0,0,0);
}
DI f32x16 ex2(f32x16 s){
#pragma unroll
  for(int e=0;e<16;e++) s[e]=__builtin_amdgcn_exp2f(s[e]);
  return s;
}
DI float rowmax(const f32x16& s){
  float m=fmaxf(fmaxf(s[0],s[1]),s[2]);
#pragma unroll
  for(int e=3;e<15;e+=2) m=fmaxf(fmaxf(m,s[e]),s[e+1]);
  m=fmaxf(m,s[15]);
  auto w=__builtin_amdgcn_permlane32_swap(__builtin_bit_cast(u32,m),__builtin_bit_cast(u32,m),false,false);
  return fmaxf(__builtin_bit_cast(float,w[0]),__builtin_bit_cast(float,w[1]));
}
DI void wait_tags(const char* base,u32 off,bool active,u64 tag){
  for(u32 it=0;it<(1u<<22);it++){
    u64 v=active?__hip_atomic_load((const GAS u64*)(base+off),RLX_AGENT):tag;
    if(__all(v==tag)) break;
    __builtin_amdgcn_s_sleep(1);
  }
  asm volatile("":::"memory");
}

__global__ void __launch_bounds__(1024) fused(Params p){
  extern __shared__ __attribute__((aligned(16))) char smem[];
  const int bid=blockIdx.x, xcd=bid&7, idx=bid>>3;
  const int br=xcd>>2, cl=idx>>3, g=idx&7, ch=(xcd&3)*4+cl;
  const int lane=threadIdx.x&63, wv=__builtin_amdgcn_readfirstlane(threadIdx.x>>6);
  const int role=wv>>2, l=wv&3;
  const int r=lane&31, h=lane>>5, c=lane&15, gq=lane>>4;
  char* ctl=p.ws+W_CTL+(br*16+ch)*2048;
  char* kvimg=p.ws+W_KV+(size_t)((br*16+ch)*8+g)*36864;
  ST(0);
  u64 tagv=0;
  if(role==0){
    const int rot=(g+2*cl)%9;
    const char* xb=p.x[br]+(size_t)(ch*128)*1152;
    const char* wb=p.w[br]+g*144;
    const u32 xo=(l*8+(lane>>3))*1152+(((lane&7)^(lane>>3))*16);
    auto kb=[&](int m){ int v=m+rot; return v>=9?v-9:v; };
    auto ldx=[&](int m){ const char* xk=xb+kb(m)*128; const int xs=m&3; const u32 d=(xs<2?xs*16384:L_X2+(xs-2)*16384)+l*1024;
#pragma unroll
      for(int j=0;j<4;j++) dma16(xo+j*32*1152,xk,d+4096*j); };
    if(l==0 && lane<3 && ch+lane<16) asm volatile("global_load_dwordx2 %0, %1, %2 sc1":"+v"(tagv):"v"(lane*2048),"s"(ctl):"memory");
    ldx(0);
    u32 wo[3];
#pragma unroll
    for(int j=0;j<3;j++){ int q=(l*3+j)*64+lane; int rho=(q*7282)>>16, c9=q-rho*9; int kk=(rho&~7)+4*(rho&1)+((rho>>1)&3); wo[j]=kk*3456+c9*16; }
    auto ldw=[&](int m,int th,u32 dst){ if(l<3){ const char* wk=wb+(size_t)kb(m)*64*3456+th*1152;
#pragma unroll
      for(int j=0;j<3;j++) dma16(wo[j],wk,dst+(l*3+j)*1024); } };
    ldw(0,1,L_WKV);
    int ws=0;
#pragma nounroll
    for(int b=0;b<8;b++){
      ldw(b,2,L_WKV+ws*18432+9216); ldw(b,0,L_WQ+ws*9216);
      ws=ws==2?0:ws+1;
      ldx(b+1); ldw(b+1,1,L_WKV+ws*18432);
      if(l<3) vmwait<7>(); else vmwait<4>();
      bar();
      if(b==0) ST(1);
    }
    ldw(8,2,L_WKV+2*18432+9216); ldw(8,0,L_WQ+2*9216);
    asm volatile("s_waitcnt vmcnt(0)":"+v"(tagv)::"memory");
    bar();
    tagv+=1;
    if(l==3 && lane<32) *(LAS u32x4*)(size_t)(L_C+lane*16)=u32x4{0x3f80u,0,0,0};
  } else {
    const u32 xr0=(l*32+c)*128+((gq^(c&7))*16);
    const u32 wrow=(8*gq+2*((lane>>2)&3))*144;
    f32x4 acc[2][5];
#pragma unroll
    for(int n=0;n<5;n++){ acc[0][n]=f32x4{0,0,0,0}; acc[1][n]=f32x4{0,0,0,0}; }
    if(role==2) gemm_wave<4>(acc,L_WKV+9216+wrow+(lane&3)*8,0,18432,xr0);
    else { const bool q=role==3; const u32 base=q?L_WQ:L_WKV;
      gemm_wave<5>(acc,base+wrow+(lane&3)*8,base+wrow+((lane&1)?104:32)+((!q&&(lane&2))?9216:0),q?9216:18432,xr0); }
    ST(2);
    const float scl=role==3?0.34004483f:1.0f;
    const u32 lm=role==1?L_KO+l*TS:role==2?L_VO+(3-l)*TS:L_Q+l*TS;
    const u32 lo=(role==2?(lane^(4*h)):lane)*16;
    const u32 go=lm-L_KO+lo;
    u32x4 v0=pack_tile(acc[0][0],acc[1][0],scl), v1=pack_tile(acc[0][1],acc[1][1],scl), v2=pack_tile(acc[0][2],acc[1][2],scl), v3=pack_tile(acc[0][3],acc[1][3],scl);
    *(LAS u32x4*)(size_t)(lm+lo)=v0; *(LAS u32x4*)(size_t)(lm+lo+1024)=v1; *(LAS u32x4*)(size_t)(lm+lo+2048)=v2; *(LAS u32x4*)(size_t)(lm+lo+3072)=v3;
    u32x4 v4; u32 le=0;
    if(role!=2){ v4=pack_tile(acc[0][4],acc[1][4],scl); le=((role==1&&h)?L_VO+(3-l)*TS:lm)+4096+r*16; *(LAS u32x4*)(size_t)le=v4; }
    if(role!=3 && ch<15){
      st16c<0>(go,kvimg,v0); st16c<1024>(go,kvimg,v1); st16c<2048>(go,kvimg,v2); st16c<3072>(go,kvimg,v3);
      if(role==1) st16c<0>(le-L_KO,kvimg,v4);
    }
  }
  barl();
  ST(3);
  const int qi=role, I=ch*4+qi;
  f32x16 cd, cf, o;
#pragma unroll
  for(int e=0;e<16;e++){ const int key=(e&3)+8*(e>>2)+4*h; cd[e]=key<=r?0.f:-3e38f; cf[e]=key>r?0.f:-3e38f; o[e]=0.f; }
  const bf16x8 qm=*(LAS bf16x8*)(size_t)(L_Q+qi*TS+l*1024+lane*16);
  u32x2 qe=*(LAS u32x2*)(size_t)(L_Q+qi*TS+4096+r*16+(l>>1)*8);
  if(h||(l&1)) qe[0]=0;
  if(h||!(l&1)) qe[1]=0;
  const u32 km0=(8+qi)*TS+l*1024+lane*16;
  const u32 kx0=h?L_C:(8+qi)*TS+4096+r*16+(l>>1)*8, kxs=h?0:TS;
  const int pp=lane&3, qq=(lane>>2)&3;
  const bool vconst=(gq&1)&&pp;
  const u32 va0=vconst?L_C+(pp==1?8:0):(15-qi)*TS+((gq&1)?4096+(4*h+qq)*16+(l>>1)*8:l*1024+(pp>>1)*512+(4*(h^(pp>>1))+qq)*16+(pp&1)*8), vas=vconst?0:TS;
  const s16x4 qx0=__builtin_bit_cast(s16x4,qe);
  {
    const float m=rowmax(qk(km0,kx0,qm,qx0,cd));
    if(h) qe[0]=pk(-m,0.f);
  }
  const s16x4 qx=__builtin_bit_cast(s16x4,qe);
  u32 km=km0,kx=kx0,va=va0;
  const f32x16 zero={0,0,0,0,0,0,0,0,0,0,0,0,0,0,0,0};
  o=pv(va,ex2(qk(km,kx,qm,qx,cd)),o);
#pragma nounroll
  for(int t=0;t<qi;t++){ km-=TS; kx-=kxs; va+=vas; o=pv(va,ex2(qk(km,kx,qm,qx,zero)),o); }
  ST(4);
  vmwait<0>(); bar();
  if(wv==0){
    if((lane==1||lane==2)&&ch+lane<16) __hip_atomic_store((GAS u64*)(ctl+lane*2048+256+g*128+(lane-1)*8),tagv,RLX_AGENT);
    const u64 tag=__builtin_bit_cast(u64,u32x2{(u32)__builtin_amdgcn_readfirstlane((u32)tagv),(u32)__builtin_amdgcn_readfirstlane((u32)(tagv>>32))});
    wait_tags(ctl,256+g*128+lane*8,lane<2&&lane<ch,tag);
  }
  bar();
  ST(5);
  for(int j=wv;j<72;j+=16){ const int d=j>=36, jj=j-36*d;
    if(d<ch){ const u32 dst=jj<18?(4-4*d)*TS+jj*1024:(16+4*d)*TS+(jj-18)*1024; dma16c(lane*16,kvimg-(size_t)(d+1)*8*36864+jj*1024,dst); } }
  vmwait<0>(); bar();
  ST(6);
  const int nmid=(I<7?I:7)-qi;
#pragma nounroll
  for(int t=0;t<nmid;t++){ km-=TS; kx-=kxs; va+=vas; o=pv(va,ex2(qk(km,kx,qm,qx,zero)),o); }
  if(I>=8){ km-=TS; kx-=kxs; va+=vas; o=pv(va,ex2(qk(km,kx,qm,qx,cf)),o); }
  if(__builtin_expect(!__all(o[12]>=0x1p-30f&&o[12]<=0x1p30f),0)){
    float m=-3e38f; o=zero; km=km0; kx=kx0; va=va0;
    const int nt=I<8?I+1:9;
#pragma nounroll
    for(int t=0;t<nt;t++){
      f32x16 cc;
#pragma unroll
      for(int e=0;e<16;e++) cc[e]=t==0?cd[e]:t==8?cf[e]:0.f;
      f32x16 s=qk(km,kx,qm,qx0,zero);
#pragma unroll
      for(int e=0;e<16;e++) s[e]=cc[e]<0.f?-__builtin_inff():s[e];
      const float mn=fmaxf(m,rowmax(s)), al=__builtin_amdgcn_exp2f(m-mn); m=mn;
#pragma unroll
      for(int e=0;e<16;e++){ o[e]*=al; s[e]-=mn; }
      o=pv(va,ex2(s),o);
      km-=TS; kx-=kxs; va+=vas;
    }
  }
  ST(7);
  char* ob=p.ws+W_O+(size_t)(br*16+ch)*147456;
  {
    const float rl=__builtin_amdgcn_rcpf(o[12]);
    const int H=g*4+l, tt=qi*2+(r>>4);
    u32x4 v={pk(o[0]*rl,o[1]*rl),pk(o[2]*rl,o[3]*rl),pk(o[4]*rl,o[5]*rl),pk(o[6]*rl,o[7]*rl)};
    st16c<0>(tt*18432+(H>>1)*1024+((H&1)*2+h)*256+c*16,ob,v);
    const u32 e=(l&1)?pk(o[10]*rl,o[11]*rl):pk(o[8]*rl,o[9]*rl);
    if(!h) __hip_atomic_store((GAS u32*)(ob+tt*18432+16384+H*64+c*4),e,RLX_AGENT);
  }
  vmwait<0>(); bar();
  ST(8);
  if(wv==0){
    if(lane==0) __hip_atomic_store((GAS u64*)(ctl+128+g*8),tagv,RLX_AGENT);
    const u64 tag=__builtin_bit_cast(u64,u32x2{(u32)__builtin_amdgcn_readfirstlane((u32)tagv),(u32)__builtin_amdgcn_readfirstlane((u32)(tagv>>32))});
    wait_tags(ctl,128+lane*8,lane<8,tag);
    if(g==0&&lane==0) __hip_atomic_store((GAS u64*)ctl,tagv,RLX_AGENT);
  } else if(wv>=7){
    const int i=wv-7, q=i*64+lane, rho=(q*7282)>>16, c9=q-rho*9;
    const int st=rho>>5, g4=(rho>>3)&3, j=4*(rho&1)+((rho>>1)&3);
    const int Hm=2*st+(g4>>1), rm=18*Hm+2*(Hm&1)+8*(j>>2)+4*(g4&1)+(j&3);
    const int He=16*st+4*(j>>1)+g4, re=18*He+((He&1)?0:16)+(j&1);
    const char* wb=p.wo[br]+g*144;
#pragma nounroll
    for(int b=0;b<8;b++) dma16(rm*1152+c9*16,wb+b*72*1152,L_WO+b*9216+i*1024);
    dma16(re*1152+c9*16,wb,L_WO+8*9216+i*1024);
    vmwait<0>();
  }
  bar();
  ST(9);
  if(wv>=8) return;
  u32x4 of[18];
  {
    const char* src=ob+wv*18432;
#pragma unroll
    for(int k=0;k<16;k++) asm volatile("global_load_dwordx4 %0, %1, %2 offset:%3 sc1":"=v"(of[k]):"v"(lane*16+(k>>2)*4096),"s"(src),"i"((k&3)*1024):"memory");
    u32 ex[8];
#pragma unroll
    for(int k=0;k<8;k++) asm volatile("global_load_dword %0, %1, %2 offset:%3 sc1":"=v"(ex[k]):"v"(lane*4+16384),"s"(src),"i"(k*256):"memory");
    asm volatile("s_waitcnt vmcnt(0)":"+v"(of[0]),"+v"(of[1]),"+v"(of[2]),"+v"(of[3]),"+v"(of[4]),"+v"(of[5]),"+v"(of[6]),"+v"(of[7]),"+v"(of[8])::"memory");
    asm volatile("":"+v"(of[9]),"+v"(of[10]),"+v"(of[11]),"+v"(of[12]),"+v"(of[13]),"+v"(of[14]),"+v"(of[15]),"+v"(ex[0]),"+v"(ex[1]),"+v"(ex[2]),"+v"(ex[3]),"+v"(ex[4]),"+v"(ex[5]),"+v"(ex[6]),"+v"(ex[7])::"memory");
    of[16]=u32x4{ex[0],ex[1],ex[2],ex[3]}; of[17]=u32x4{ex[4],ex[5],ex[6],ex[7]};
  }
  ST(10);
  f32x4 acc[5];
#pragma unroll
  for(int n=0;n<5;n++) acc[n]=f32x4{0,0,0,0};
  const u32 wa=L_WO+(8*gq+2*qq)*144+pp*8;
#pragma unroll
  for(int k=0;k<18;k++){
    const u32 a=wa+(k>=9?9*4608:0); const int ko=(k>=9?k-9:k)*4608;
#pragma unroll
    for(int n=0;n<5;n++) acc[n]=__builtin_amdgcn_mfma_f32_16x16x32_bf16(trfrag(a+ko+n*32,a+ko+n*32+144),__builtin_bit_cast(bf16x8,of[k]),acc[n],0,0,0);
  }
  ST(11);
  const u32 sb=L_ST+wv*2560;
#pragma unroll
  for(int n=0;n<5;n++) if(n<4||gq<2) *(LAS u32x2*)(size_t)(sb+c*144+n*32+gq*8)=u32x2{pk(acc[n][0],acc[n][1]),pk(acc[n][2],acc[n][3])};
  char* od=p.out+(size_t)(ch*128+wv*16)*2304+br*1152+g*144;
#pragma unroll
  for(int j=0;j<3;j++){ const int q=j*64+lane, row=(q*7282)>>16, c9=q-row*9;
    if(q<144) *(GAS u32x4*)(od+row*2304+c9*16)=*(LAS u32x4*)(size_t)(sb+q*16); }
  ST(12);
}

extern "C" void kernel_launch(void* const* d_in, const int* in_sizes, int n_in, void* d_out, int out_size, void* d_ws, size_t ws_size, hipStream_t stream){
  Params p;
  p.x[0]=(const char*)d_in[0]; p.w[0]=(const char*)d_in[1]; p.wo[0]=(const char*)d_in[2];
  p.x[1]=(const char*)d_in[3]; p.w[1]=(const char*)d_in[4]; p.wo[1]=(const char*)d_in[5];
  p.out=(char*)d_out; p.ws=(char*)d_ws;
  (void)hipFuncSetAttribute((const void*)fused,hipFuncAttributeMaxDynamicSharedMemorySize,LDS_BYTES);
  fused<<<256,1024,LDS_BYTES,stream>>>(p);
}
```

```cpp
#define SPIN_AT 12
#define SPIN_US 45
#include <hip/hip_runtime.h>
typedef unsigned u32;
typedef unsigned long long u64;
typedef u32 u32x2 __attribute__((ext_vector_type(2)));
typedef u32 u32x4 __attribute__((ext_vector_type(4)));
typedef short s16x4 __attribute__((ext_vector_type(4)));
typedef __bf16 bf16x2 __attribute__((ext_vector_type(2)));
typedef __bf16 bf16x8 __attribute__((ext_vector_type(8)));
typedef float f32x2 __attribute__((ext_vector_type(2)));
typedef float f32x4 __attribute__((ext_vector_type(4)));
typedef float f32x16 __attribute__((ext_vector_type(16)));
#define LAS __attribute__((address_space(3)))
#define GAS __attribute__((address_space(1)))
#define DI __device__ __forceinline__
#define RLX_AGENT __ATOMIC_RELAXED, __HIP_MEMORY_SCOPE_AGENT

DI void dma16(u32 voff, const char* sbase, u32 lds){
  asm volatile("s_mov_b32 m0, %2\n\ts_nop 0\n\tglobal_load_lds_dwordx4 %0, %1"::"v"(voff),"s"(sbase),"s"(lds):"memory");
}
DI void dma16c(u32 voff, const char* sbase, u32 lds){
  asm volatile("s_mov_b32 m0, %2\n\ts_nop 0\n\tglobal_load_lds_dwordx4 %0, %1 sc1"::"v"(voff),"s"(sbase),"s"(lds):"memory");
}
template<int N> DI void vmwait(){ asm volatile("s_waitcnt vmcnt(%0)"::"i"(N):"memory"); }
DI void bar(){ asm volatile("":::"memory"); __builtin_amdgcn_s_barrier(); asm volatile("":::"memory"); }
DI void barl(){ asm volatile("s_waitcnt lgkmcnt(0)":::"memory"); __builtin_amdgcn_s_barrier(); asm volatile("":::"memory"); }
DI u32 pk(float a,float b){ f32x2 v={a,b}; return __builtin_bit_cast(u32,__builtin_convertvector(v,bf16x2)); }
DI bf16x8 trfrag(u32 a0,u32 a1){
  s16x4 lo=__builtin_amdgcn_ds_read_tr16_b64_v4i16((LAS s16x4*)(size_t)a0);
  s16x4 hi=__builtin_amdgcn_ds_read_tr16_b64_v4i16((LAS s16x4*)(size_t)a1);
  return __builtin_bit_cast(bf16x8,__builtin_shufflevector(lo,hi,0,1,2,3,4,5,6,7));
}
template<int OFF> DI void st16c(u32 voff,char* base,u32x4 v){ asm volatile("global_store_dwordx4 %0, %1, %2 offset:%3 sc1\n\ts_nop 1"::"v"(voff),"v"(v),"s"(base),"i"(OFF):"memory"); }
DI u64 rt_(){ u64 t; asm volatile("s_memrealtime %0\n s_waitcnt lgkmcnt(0)":"=s"(t)::"memory"); return t; }
#define ST(i) do{ if((i)==0) t_start=rt_(); if((i)==SPIN_AT){ while(rt_()<t_start+SPIN_US*100) __builtin_amdgcn_s_sleep(1); } }while(0)

constexpr int PD=2;
constexpr int TS=4608;
constexpr int L_KO=8*TS, L_VO=12*TS;
constexpr int L_WKV=L_KO;
constexpr int L_WQ=L_WKV+3*18432;
constexpr int L_X2=L_WQ+3*9216;
constexpr int L_Q=L_X2;
constexpr int L_C=163328;
constexpr int L_WO=0, L_ST=88*1024;
constexpr int LDS_BYTES=163840;
constexpr size_t W_CTL=0, W_KV=1u<<20, W_O=16u<<20;

struct Params { const char* x[2]; const char* w[2]; const char* wo[2]; char* out; char* ws; };

template<int NT> DI void gemm_wave(f32x4 (&acc)[2][5], u32 wm0,u32 we0,int wstride,u32 xr0){
  bf16x8 wf[PD+1], xf[2][2];
  constexpr int NU=2*NT;
  int ws=0, xs=0;
  u32 wm,we;
  auto R=[&](int u){ const int ks=u/NT,n=u%NT; const u32 ko=ks*4608; const u32 co=n==0?0:n==1?40:n==2?72:112;
    if(n<4) wf[u%(PD+1)]=trfrag(wm+ko+co,wm+ko+co+144); else wf[u%(PD+1)]=trfrag(we+ko,we+ko+144); };
#pragma nounroll
  for(int b=0;b<9;b++){
    bar();
    wm=wm0+ws*wstride; we=we0+ws*wstride; ws=ws==2?0:ws+1;
    const u32 xa=xr0+(xs<2?xs*16384:L_X2+(xs-2)*16384); xs=(xs+1)&3;
    xf[0][0]=*(LAS bf16x8*)(size_t)(xa); xf[0][1]=*(LAS bf16x8*)(size_t)(xa+2048);
#pragma unroll
    for(int u=0;u<PD;u++) R(u);
    xf[1][0]=*(LAS bf16x8*)(size_t)(xa^64); xf[1][1]=*(LAS bf16x8*)(size_t)((xa^64)+2048);
    __builtin_amdgcn_sched_barrier(0);
#pragma unroll
    for(int u=0;u<NU;u++){
      if(u+PD<NU) R(u+PD);
      acc[0][u%NT]=__builtin_amdgcn_mfma_f32_16x16x32_bf16(wf[u%(PD+1)],xf[u/NT][0],acc[0][u%NT],0,0,0);
      acc[1][u%NT]=__builtin_amdgcn_mfma_f32_16x16x32_bf16(wf[u%(PD+1)],xf[u/NT][1],acc[1][u%NT],0,0,0);
      __builtin_amdgcn_sched_group_barrier(8,1,0); __builtin_amdgcn_sched_group_barrier(0x100,1,0); __builtin_amdgcn_sched_group_barrier(8,1,0); __builtin_amdgcn_sched_group_barrier(0x100,1,0);
      __builtin_amdgcn_sched_barrier(0);
    }
  }
}
DI u32x4 pack_tile(const f32x4& a,const f32x4& b,float s){
  u32 x0=pk(a[0]*s,a[1]*s), x1=pk(a[2]*s,a[3]*s), y0=pk(b[0]*s,b[1]*s), y1=pk(b[2]*s,b[3]*s);
  auto s0=__builtin_amdgcn_permlane16_swap(x0,y0,false,false); auto s1=__builtin_amdgcn_permlane16_swap(x1,y1,false,false);
  return u32x4{s0[0],s1[0],s0[1],s1[1]};
}
DI f32x16 qk(u32 km,u32 kx,bf16x8 qm,s16x4 qx,f32x16 c){
  s16x4 a=*(LAS s16x4*)(size_t)kx; bf16x8 b=*(LAS bf16x8*)(size_t)km;
  c=__builtin_amdgcn_mfma_f32_32x32x8bf16_1k(a,qx,c,0,0,0);
  return __builtin_amdgcn_mfma_f32_32x32x16_bf16(b,qm,c,0,0,0);
}
DI f32x16 pv(u32 va,const f32x16& pr,f32x16 o){
  bf16x8 v0=trfrag(va,va+128), v1=trfrag(va+256,va+384);
  u32x4 p0={pk(pr[0],pr[1]),pk(pr[2],pr[3]),pk(pr[4],pr[5]),pk(pr[6],pr[7])};
  u32x4 p1={pk(pr[8],pr[9]),pk(pr[10],pr[11]),pk(pr[12],pr[13]),pk(pr[14],pr[15])};
  o=__builtin_amdgcn_mfma_f32_32x32x16_bf16(v0,__builtin_bit_cast(bf16x8,p0),o,0,0,0);
  return __builtin_amdgcn_mfma_f32_32x32x16_bf16(v1,__builtin_bit_cast(bf16x8,p1),o,0,0,0);
}
DI f32x16 ex2(f32x16 s){
#pragma unroll
  for(int e=0;e<16;e++) s[e]=__builtin_amdgcn_exp2f(s[e]);
  return s;
}
DI float rowmax(const f32x16& s){
  float m=fmaxf(fmaxf(s[0],s[1]),s[2]);
#pragma unroll
  for(int e=3;e<15;e+=2) m=fmaxf(fmaxf(m,s[e]),s[e+1]);
  m=fmaxf(m,s[15]);
  auto w=__builtin_amdgcn_permlane32_swap(__builtin_bit_cast(u32,m),__builtin_bit_cast(u32,m),false,false);
  return fmaxf(__builtin_bit_cast(float,w[0]),__builtin_bit_cast(float,w[1]));
}
DI void wait_tags(const char* base,u32 off,bool active,u64 tag){
  for(u32 it=0;it<(1u<<22);it++){
    u64 v=active?__hip_atomic_load((const GAS u64*)(base+off),RLX_AGENT):tag;
    if(__all(v==tag)) break;
    __builtin_amdgcn_s_sleep(1);
  }
  asm volatile("":::"memory");
}

__global__ void __launch_bounds__(1024) fused(Params p){
  extern __shared__ __attribute__((aligned(16))) char smem[];
  const int bid=blockIdx.x, xcd=bid&7, idx=bid>>3;
  const int br=xcd>>2, cl=idx>>3, g=idx&7, ch=(xcd&3)*4+cl;
  const int lane=threadIdx.x&63, wv=__builtin_amdgcn_readfirstlane(threadIdx.x>>6);
  const int role=wv>>2, l=wv&3;
  const int r=lane&31, h=lane>>5, c=lane&15, gq=lane>>4;
  char* ctl=p.ws+W_CTL+(br*16+ch)*2048;
  char* kvimg=p.ws+W_KV+(size_t)((br*16+ch)*8+g)*36864;
  u64 t_start=0; (void)t_start;
  ST(0);
  u64 tagv=0;
  if(role==0){
    const int rot=(g+2*cl)%9;
    const char* xb=p.x[br]+(size_t)(ch*128)*1152;
    const char* wb=p.w[br]+g*144;
    const u32 xo=(l*8+(lane>>3))*1152+(((lane&7)^(lane>>3))*16);
    auto kb=[&](int m){ int v=m+rot; return v>=9?v-9:v; };
    auto ldx=[&](int m){ const char* xk=xb+kb(m)*128; const int xs=m&3; const u32 d=(xs<2?xs*16384:L_X2+(xs-2)*16384)+l*1024;
#pragma unroll
      for(int j=0;j<4;j++) dma16(xo+j*32*1152,xk,d+4096*j); };
    if(l==0 && lane<3 && ch+lane<16) asm volatile("global_load_dwordx2 %0, %1, %2 sc1":"+v"(tagv):"v"(lane*2048),"s"(ctl):"memory");
    ldx(0);
    u32 wo[3];
#pragma unroll
    for(int j=0;j<3;j++){ int q=(l*3+j)*64+lane; int rho=(q*7282)>>16, c9=q-rho*9; int kk=(rho&~7)+4*(rho&1)+((rho>>1)&3); wo[j]=kk*3456+c9*16; }
    auto ldw=[&](int m,int th,u32 dst){ if(l<3){ const char* wk=wb+(size_t)kb(m)*64*3456+th*1152;
#pragma unroll
      for(int j=0;j<3;j++) dma16(wo[j],wk,dst+(l*3+j)*1024); } };
    ldw(0,1,L_WKV);
    int ws=0;
#pragma nounroll
    for(int b=0;b<8;b++){
      ldw(b,2,L_WKV+ws*18432+9216); ldw(b,0,L_WQ+ws*9216);
      ws=ws==2?0:ws+1;
      ldx(b+1); ldw(b+1,1,L_WKV+ws*18432);
      if(l<3) vmwait<7>(); else vmwait<4>();
      bar();
      if(b==0) ST(1);
    }
    ldw(8,2,L_WKV+2*18432+9216); ldw(8,0,L_WQ+2*9216);
    asm volatile("s_waitcnt vmcnt(0)":"+v"(tagv)::"memory");
    bar();
    tagv+=1;
    if(l==3 && lane<32) *(LAS u32x4*)(size_t)(L_C+lane*16)=u32x4{0x3f80u,0,0,0};
  } else {
    const u32 xr0=(l*32+c)*128+((gq^(c&7))*16);
    const u32 wrow=(8*gq+2*((lane>>2)&3))*144;
    f32x4 acc[2][5];
#pragma unroll
    for(int n=0;n<5;n++){ acc[0][n]=f32x4{0,0,0,0}; acc[1][n]=f32x4{0,0,0,0}; }
    if(role==2) gemm_wave<4>(acc,L_WKV+9216+wrow+(lane&3)*8,0,18432,xr0);
    else { const bool q=role==3; const u32 base=q?L_WQ:L_WKV;
      gemm_wave<5>(acc,base+wrow+(lane&3)*8,base+wrow+((lane&1)?104:32)+((!q&&(lane&2))?9216:0),q?9216:18432,xr0); }
    ST(2);
    const float scl=role==3?0.34004483f:1.0f;
    const u32 lm=role==1?L_KO+l*TS:role==2?L_VO+(3-l)*TS:L_Q+l*TS;
    const u32 lo=(role==2?(lane^(4*h)):lane)*16;
    const u32 go=lm-L_KO+lo;
    u32x4 v0=pack_tile(acc[0][0],acc[1][0],scl), v1=pack_tile(acc[0][1],acc[1][1],scl), v2=pack_tile(acc[0][2],acc[1][2],scl), v3=pack_tile(acc[0][3],acc[1][3],scl);
    *(LAS u32x4*)(size_t)(lm+lo)=v0; *(LAS u32x4*)(size_t)(lm+lo+1024)=v1; *(LAS u32x4*)(size_t)(lm+lo+2048)=v2; *(LAS u32x4*)(size_t)(lm+lo+3072)=v3;
    u32x4 v4; u32 le=0;
    if(role!=2){ v4=pack_tile(acc[0][4],acc[1][4],scl); le=((role==1&&h)?L_VO+(3-l)*TS:lm)+4096+r*16; *(LAS u32x4*)(size_t)le=v4; }
    if(role!=3 && ch<15){
      st16c<0>(go,kvimg,v0); st16c<1024>(go,kvimg,v1); st16c<2048>(go,kvimg,v2); st16c<3072>(go,kvimg,v3);
      if(role==1) st16c<0>(le-L_KO,kvimg,v4);
    }
  }
  barl();
  ST(3);
  const int qi=role, I=ch*4+qi;
  f32x16 cd, cf, o;
#pragma unroll
  for(int e=0;e<16;e++){ const int key=(e&3)+8*(e>>2)+4*h; cd[e]=key<=r?0.f:-3e38f; cf[e]=key>r?0.f:-3e38f; o[e]=0.f; }
  const bf16x8 qm=*(LAS bf16x8*)(size_t)(L_Q+qi*TS+l*1024+lane*16);
  u32x2 qe=*(LAS u32x2*)(size_t)(L_Q+qi*TS+4096+r*16+(l>>1)*8);
  if(h||(l&1)) qe[0]=0;
  if(h||!(l&1)) qe[1]=0;
  const u32 km0=(8+qi)*TS+l*1024+lane*16;
  const u32 kx0=h?L_C:(8+qi)*TS+4096+r*16+(l>>1)*8, kxs=h?0:TS;
  const int pp=lane&3, qq=(lane>>2)&3;
  const bool vconst=(gq&1)&&pp;
  const u32 va0=vconst?L_C+(pp==1?8:0):(15-qi)*TS+((gq&1)?4096+(4*h+qq)*16+(l>>1)*8:l*1024+(pp>>1)*512+(4*(h^(pp>>1))+qq)*16+(pp&1)*8), vas=vconst?0:TS;
  const s16x4 qx0=__builtin_bit_cast(s16x4,qe);
  {
    const float m=rowmax(qk(km0,kx0,qm,qx0,cd));
    if(h) qe[0]=pk(-m,0.f);
  }
  const s16x4 qx=__builtin_bit_cast(s16x4,qe);
  u32 km=km0,kx=kx0,va=va0;
  const f32x16 zero={0,0,0,0,0,0,0,0,0,0,0,0,0,0,0,0};
  o=pv(va,ex2(qk(km,kx,qm,qx,cd)),o);
#pragma nounroll
  for(int t=0;t<qi;t++){ km-=TS; kx-=kxs; va+=vas; o=pv(va,ex2(qk(km,kx,qm,qx,zero)),o); }
  ST(4);
  vmwait<0>(); bar();
  if(wv==0){
    if((lane==1||lane==2)&&ch+lane<16) __hip_atomic_store((GAS u64*)(ctl+lane*2048+256+g*128+(lane-1)*8),tagv,RLX_AGENT);
    const u64 tag=__builtin_bit_cast(u64,u32x2{(u32)__builtin_amdgcn_readfirstlane((u32)tagv),(u32)__builtin_amdgcn_readfirstlane((u32)(tagv>>32))});
    wait_tags(ctl,256+g*128+lane*8,lane<2&&lane<ch,tag);
  }
  bar();
  ST(5);
  for(int j=wv;j<72;j+=16){ const int d=j>=36, jj=j-36*d;
    if(d<ch){ const u32 dst=jj<18?(4-4*d)*TS+jj*1024:(16+4*d)*TS+(jj-18)*1024; dma16c(lane*16,kvimg-(size_t)(d+1)*8*36864+jj*1024,dst); } }
  vmwait<0>(); bar();
  ST(6);
  const int nmid=(I<7?I:7)-qi;
#pragma nounroll
  for(int t=0;t<nmid;t++){ km-=TS; kx-=kxs; va+=vas; o=pv(va,ex2(qk(km,kx,qm,qx,zero)),o); }
  if(I>=8){ km-=TS; kx-=kxs; va+=vas; o=pv(va,ex2(qk(km,kx,qm,qx,cf)),o); }
  if(__builtin_expect(!__all(o[12]>=0x1p-30f&&o[12]<=0x1p30f),0)){
    float m=-3e38f; o=zero; km=km0; kx=kx0; va=va0;
    const int nt=I<8?I+1:9;
#pragma nounroll
    for(int t=0;t<nt;t++){
      f32x16 cc;
#pragma unroll
      for(int e=0;e<16;e++) cc[e]=t==0?cd[e]:t==8?cf[e]:0.f;
      f32x16 s=qk(km,kx,qm,qx0,zero);
#pragma unroll
      for(int e=0;e<16;e++) s[e]=cc[e]<0.f?-__builtin_inff():s[e];
      const float mn=fmaxf(m,rowmax(s)), al=__builtin_amdgcn_exp2f(m-mn); m=mn;
#pragma unroll
      for(int e=0;e<16;e++){ o[e]*=al; s[e]-=mn; }
      o=pv(va,ex2(s),o);
      km-=TS; kx-=kxs; va+=vas;
    }
  }
  ST(7);
  char* ob=p.ws+W_O+(size_t)(br*16+ch)*147456;
  {
    const float rl=__builtin_amdgcn_rcpf(o[12]);
    const int H=g*4+l, tt=qi*2+(r>>4);
    u32x4 v={pk(o[0]*rl,o[1]*rl),pk(o[2]*rl,o[3]*rl),pk(o[4]*rl,o[5]*rl),pk(o[6]*rl,o[7]*rl)};
    st16c<0>(tt*18432+(H>>1)*1024+((H&1)*2+h)*256+c*16,ob,v);
    const u32 e=(l&1)?pk(o[10]*rl,o[11]*rl):pk(o[8]*rl,o[9]*rl);
    if(!h) __hip_atomic_store((GAS u32*)(ob+tt*18432+16384+H*64+c*4),e,RLX_AGENT);
  }
  vmwait<0>(); bar();
  ST(8);
  if(wv==0){
    if(lane==0) __hip_atomic_store((GAS u64*)(ctl+128+g*8),tagv,RLX_AGENT);
    const u64 tag=__builtin_bit_cast(u64,u32x2{(u32)__builtin_amdgcn_readfirstlane((u32)tagv),(u32)__builtin_amdgcn_readfirstlane((u32)(tagv>>32))});
    wait_tags(ctl,128+lane*8,lane<8,tag);
    if(g==0&&lane==0) __hip_atomic_store((GAS u64*)ctl,tagv,RLX_AGENT);
  } else if(wv>=7){
    const int i=wv-7, q=i*64+lane, rho=(q*7282)>>16, c9=q-rho*9;
    const int st=rho>>5, g4=(rho>>3)&3, j=4*(rho&1)+((rho>>1)&3);
    const int Hm=2*st+(g4>>1), rm=18*Hm+2*(Hm&1)+8*(j>>2)+4*(g4&1)+(j&3);
    const int He=16*st+4*(j>>1)+g4, re=18*He+((He&1)?0:16)+(j&1);
    const char* wb=p.wo[br]+g*144;
#pragma nounroll
    for(int b=0;b<8;b++) dma16(rm*1152+c9*16,wb+b*72*1152,L_WO+b*9216+i*1024);
    dma16(re*1152+c9*16,wb,L_WO+8*9216+i*1024);
    vmwait<0>();
  }
  bar();
  ST(9);
  if(wv>=8) return;
  u32x4 of[18];
  {
    const char* src=ob+wv*18432;
#pragma unroll
    for(int k=0;k<16;k++) asm volatile("global_load_dwordx4 %0, %1, %2 offset:%3 sc1":"=v"(of[k]):"v"(lane*16+(k>>2)*4096),"s"(src),"i"((k&3)*1024):"memory");
    u32 ex[8];
#pragma unroll
    for(int k=0;k<8;k++) asm volatile("global_load_dword %0, %1, %2 offset:%3 sc1":"=v"(ex[k]):"v"(lane*4+16384),"s"(src),"i"(k*256):"memory");
    asm volatile("s_waitcnt vmcnt(0)":"+v"(of[0]),"+v"(of[1]),"+v"(of[2]),"+v"(of[3]),"+v"(of[4]),"+v"(of[5]),"+v"(of[6]),"+v"(of[7]),"+v"(of[8])::"memory");
    asm volatile("":"+v"(of[9]),"+v"(of[10]),"+v"(of[11]),"+v"(of[12]),"+v"(of[13]),"+v"(of[14]),"+v"(of[15]),"+v"(ex[0]),"+v"(ex[1]),"+v"(ex[2]),"+v"(ex[3]),"+v"(ex[4]),"+v"(ex[5]),"+v"(ex[6]),"+v"(ex[7])::"memory");
    of[16]=u32x4{ex[0],ex[1],ex[2],ex[3]}; of[17]=u32x4{ex[4],ex[5],ex[6],ex[7]};
  }
  ST(10);
  f32x4 acc[5];
#pragma unroll
  for(int n=0;n<5;n++) acc[n]=f32x4{0,0,0,0};
  const u32 wa=L_WO+(8*gq+2*qq)*144+pp*8;
#pragma unroll
  for(int k=0;k<18;k++){
    const u32 a=wa+(k>=9?9*4608:0); const int ko=(k>=9?k-9:k)*4608;
#pragma unroll
    for(int n=0;n<5;n++) acc[n]=__builtin_amdgcn_mfma_f32_16x16x32_bf16(trfrag(a+ko+n*32,a+ko+n*32+144),__builtin_bit_cast(bf16x8,of[k]),acc[n],0,0,0);
  }
  ST(11);
  const u32 sb=L_ST+wv*2560;
#pragma unroll
  for(int n=0;n<5;n++) if(n<4||gq<2) *(LAS u32x2*)(size_t)(sb+c*144+n*32+gq*8)=u32x2{pk(acc[n][0],acc[n][1]),pk(acc[n][2],acc[n][3])};
  char* od=p.out+(size_t)(ch*128+wv*16)*2304+br*1152+g*144;
#pragma unroll
  for(int j=0;j<3;j++){ const int q=j*64+lane, row=(q*7282)>>16, c9=q-row*9;
    if(q<144) st16c<0>(row*2304+c9*16,od,*(LAS u32x4*)(size_t)(sb+q*16)); }
  ST(12);
}

extern "C" void kernel_launch(void* const* d_in, const int* in_sizes, int n_in, void* d_out, int out_size, void* d_ws, size_t ws_size, hipStream_t stream){
  Params p;
  p.x[0]=(const char*)d_in[0]; p.w[0]=(const char*)d_in[1]; p.wo[0]=(const char*)d_in[2];
  p.x[1]=(const char*)d_in[3]; p.w[1]=(const char*)d_in[4]; p.wo[1]=(const char*)d_in[5];
  p.out=(char*)d_out; p.ws=(char*)d_ws;
  (void)hipFuncSetAttribute((const void*)fused,hipFuncAttributeMaxDynamicSharedMemorySize,LDS_BYTES);
  fused<<<256,1024,LDS_BYTES,stream>>>(p);
}
```

```cpp
#include <hip/hip_runtime.h>
typedef unsigned u32;
typedef unsigned long long u64;
typedef u32 u32x2 __attribute__((ext_vector_type(2)));
typedef u32 u32x4 __attribute__((ext_vector_type(4)));
typedef short s16x4 __attribute__((ext_vector_type(4)));
typedef __bf16 bf16x2 __attribute__((ext_vector_type(2)));
typedef __bf16 bf16x8 __attribute__((ext_vector_type(8)));
typedef float f32x2 __attribute__((ext_vector_type(2)));
typedef float f32x16 __attribute__((ext_vector_type(16)));
#define LAS __attribute__((address_space(3)))
#define GAS __attribute__((address_space(1)))
#define DI __device__ __forceinline__
#define RLX_AGENT __ATOMIC_RELAXED, __HIP_MEMORY_SCOPE_AGENT
#define MF32(a,b,c) __builtin_amdgcn_mfma_f32_32x32x16_bf16(a,b,c,0,0,0)
#define MF8(a,b,c) __builtin_amdgcn_mfma_f32_32x32x8bf16_1k(a,b,c,0,0,0)
constexpr int KVS=36992;
constexpr int OHS=73856;
constexpr int TS=4608;
constexpr int S_TAG=158720;
constexpr int S_DR=158880;
constexpr int S_O=158892;
constexpr int LDS_BYTES=163840;
constexpr size_t W_CTL=0, W_KV=1u<<20, W_O=16u<<20;
struct Branch { const void *x,*w,*wo; char *out,*ctl,*kv,*o,*spare; };
struct Params { Branch b[2]; u64 order[32]; };
#define MAIN_ASM \
  "s_lshl_b32 s36, s2, 4\n" \
  "s_and_b32 s36, s36, 64\n" \
  "s_load_dwordx16 s[16:31], s[0:1], s36\n" \
  "s_and_b32 s37, s2, 0xf8\n" \
  "s_add_u32 s37, s37, 128\n" \
  "s_load_dwordx2 s[12:13], s[0:1], s37\n" \
  "v_readfirstlane_b32 s3, v0\n" \
  "v_and_b32 v124, 63, v0\n" \
  "s_lshr_b32 s9, s2, 3\n" \
  "s_and_b32 s6, s9, 7\n" \
  "s_lshr_b32 s8, s2, 6\n" \
  "s_and_b32 s7, s2, 3\n" \
  "s_lshl_b32 s7, s7, 2\n" \
  "s_add_u32 s7, s7, s8\n" \
  "s_lshr_b32 s3, s3, 6\n" \
  "s_and_b32 s4, s3, 3\n" \
  "s_lshr_b32 s5, s3, 2\n" \
  "s_and_b32 s10, s5, 1\n" \
  "s_cmp_lg_u32 s3, 3\n" \
  "s_cbranch_scc1 Lpf_%=\n" \
  "s_getpc_b64 s[70:71]\n" \
    \
  "Lpc_%=:\n" \
  "s_and_b32 s36, s9, 15\n" \
  "s_lshl_b32 s36, s36, 7\n" \
  "v_lshl_add_u32 v12, v124, 11, s36\n" \
  "v_min_u32 v12, Lend_%=-Lpc_%=, v12\n" \
  "s_mov_b64 exec, 0x1ff\n" \
  "global_load_dword v13, v12, s[70:71]\n" \
  "s_mov_b64 exec, -1\n" \
    \
  "Lpf_%=:\n" \
  "s_cmp_lg_u32 s5, 0\n" \
  "s_cbranch_scc1 Lcomp_%=\n" \
  "s_setprio 3\n" \
  "v_lshrrev_b32 v8, 3, v124\n" \
  "v_and_b32 v9, 7, v124\n" \
  "v_xor_b32 v9, v9, v8\n" \
  "v_mul_u32_u24 v8, 1152, v8\n" \
  "v_lshl_add_u32 v1, v9, 4, v8\n" \
  "v_add_u32 v2, 8192, v1\n" \
  "v_add_u32 v3, 16384, v1\n" \
  "v_add_u32 v4, 24576, v1\n" \
  "s_mul_i32 s84, s4, 3072\n" \
  "s_mul_i32 s36, s4, 192\n" \
  "v_lshl_add_u32 v11, v124, 4, s84\n" \
  "s_add_u32 s37, s36, 0\n" \
  "v_add_u32 v8, s37, v124\n" \
  "v_mul_u32_u24 v8, 7282, v8\n" \
  "v_lshrrev_b32 v8, 16, v8\n" \
  "v_and_b32 v9, 0x78, v8\n" \
  "v_and_b32 v10, 1, v8\n" \
  "v_lshl_add_u32 v9, v10, 2, v9\n" \
  "v_bfe_u32 v10, v8, 1, 2\n" \
  "v_add_u32 v9, v9, v10\n" \
  "v_mul_u32_u24 v9, 3456, v9\n" \
  "v_mul_u32_u24 v8, 144, v8\n" \
  "v_sub_u32 v9, v9, v8\n" \
  "v_add_u32 v5, v9, v11\n" \
  "s_add_u32 s37, s36, 64\n" \
  "v_add_u32 v8, s37, v124\n" \
  "v_mul_u32_u24 v8, 7282, v8\n" \
  "v_lshrrev_b32 v8, 16, v8\n" \
  "v_and_b32 v9, 0x78, v8\n" \
  "v_and_b32 v10, 1, v8\n" \
  "v_lshl_add_u32 v9, v10, 2, v9\n" \
  "v_bfe_u32 v10, v8, 1, 2\n" \
  "v_add_u32 v9, v9, v10\n" \
  "v_mul_u32_u24 v9, 3456, v9\n" \
  "v_mul_u32_u24 v8, 144, v8\n" \
  "v_sub_u32 v9, v9, v8\n" \
  "v_add_u32 v6, v9, v11\n" \
  "s_add_u32 s37, s36, 128\n" \
  "v_add_u32 v8, s37, v124\n" \
  "v_mul_u32_u24 v8, 7282, v8\n" \
  "v_lshrrev_b32 v8, 16, v8\n" \
  "v_and_b32 v9, 0x78, v8\n" \
  "v_and_b32 v10, 1, v8\n" \
  "v_lshl_add_u32 v9, v10, 2, v9\n" \
  "v_bfe_u32 v10, v8, 1, 2\n" \
  "v_add_u32 v9, v9, v10\n" \
  "v_mul_u32_u24 v9, 3456, v9\n" \
  "v_mul_u32_u24 v8, 144, v8\n" \
  "v_sub_u32 v9, v9, v8\n" \
  "v_add_u32 v7, v9, v11\n" \
  "s_lshl_b32 s36, s7, 2\n" \
  "s_add_u32 s36, s36, s4\n" \
  "s_mul_i32 s36, s36, 36864\n" \
  "s_mul_i32 s37, s6, 144\n" \
  "s_lshl_b32 s80, s4, 12\n" \
  "s_add_u32 s81, s84, 36864\n" \
  "s_and_b32 s85, s2, 3\n" \
  "s_mul_i32 s85, s85, -3\n" \
  "s_mov_b32 m0, s80\n" \
  "s_waitcnt lgkmcnt(0)\n" \
  "s_add_u32 s16, s16, s36\n" \
  "s_addc_u32 s17, s17, 0\n" \
  "s_add_u32 s18, s18, s37\n" \
  "s_addc_u32 s19, s19, 0\n" \
  "s_lshr_b64 s[74:75], s[12:13], 24\n" \
  "s_and_b32 s36, s12, 15\n" \
  "s_add_u32 s36, s36, s85\n" \
  "s_add_u32 s37, s36, 9\n" \
  "s_min_u32 s36, s36, s37\n" \
  "s_lshl_b32 s37, s36, 7\n" \
  "s_add_u32 s72, s16, s37\n" \
  "s_addc_u32 s73, s17, 0\n" \
  "global_load_lds_dwordx4 v1, s[72:73]\n" \
  "global_load_lds_dwordx4 v2, s[72:73] offset:1024\n" \
  "global_load_lds_dwordx4 v3, s[72:73] offset:2048\n" \
  "global_load_lds_dwordx4 v4, s[72:73] offset:3072\n" \
  "s_mul_i32 s36, s36, 221184\n" \
  "s_lshr_b64 s[12:13], s[12:13], 4\n" \
  "s_add_u32 s76, s18, s36\n" \
  "s_addc_u32 s77, s19, 0\n" \
  "s_cmp_eq_u32 s4, 3\n" \
  "s_cbranch_scc1 Lld0_%=\n" \
  "s_mov_b32 m0, s81\n" \
  "s_add_u32 s78, s76, 1152\n" \
  "s_addc_u32 s79, s77, 0\n" \
  "global_load_lds_dwordx4 v5, s[78:79]\n" \
  "global_load_lds_dwordx4 v6, s[78:79] offset:1024\n" \
  "global_load_lds_dwordx4 v7, s[78:79] offset:2048\n" \
  "Lld0_%=:\n" \
  "v_mov_b32 v121, 158884\n" \
  "v_mov_b32 v122, 1\n" \
  "s_lshl_b32 s38, s6, 7\n" \
  "s_mul_i32 s39, s7, 41472\n" \
  "s_cmp_ge_u32 s7, 5\n" \
  "s_mov_b32 s36, 18432\n" \
  "s_cmov_b32 s36, 350208\n" \
  "s_bitcmp1_b32 0x210, s7\n" \
  "s_cselect_b32 s37, 1024, s38\n" \
  "s_add_u32 s36, s36, s37\n" \
  "s_cmp_eq_u32 s4, 2\n" \
  "s_cmov_b32 s38, 1024\n" \
  "s_add_u32 s39, s39, s38\n" \
  "s_bitcmp1_b32 128, s6\n" \
  "s_cselect_b32 s41, 3, 2\n" \
  "s_bitcmp1_b32 s4, 0\n" \
  "s_cselect_b32 s36, s36, s39\n" \
  "s_mov_b32 s54, 4608\n" \
  "s_cmov_b32 s54, 41472\n" \
  "s_mov_b32 s55, 9216\n" \
  "s_cmov_b32 s55, 41472\n" \
  "s_mov_b32 s40, 0xffff\n" \
  "s_cmov_b32 s40, 0x3ff\n" \
  "s_cmp_ge_u32 s4, s41\n" \
  "s_cmov_b32 s40, 0\n" \
  "s_lshr_b32 s56, s40, s7\n" \
  "s_mul_i32 s57, s6, 4608\n" \
  "s_add_u32 s52, s20, s36\n" \
  "s_addc_u32 s53, s21, 0\n" \
  "s_cmp_eq_u32 s4, 3\n" \
  "s_cbranch_scc1 Lldx_%=\n" \
  "s_mov_b32 s83, 0\n" \
    \
  "Lld_%=:\n" \
  "s_add_u32 m0, s81, 9216\n" \
  "s_add_u32 s78, s76, 2304\n" \
  "s_addc_u32 s79, s77, 0\n" \
  "global_load_lds_dwordx4 v5, s[78:79]\n" \
  "global_load_lds_dwordx4 v6, s[78:79] offset:1024\n" \
  "global_load_lds_dwordx4 v7, s[78:79] offset:2048\n" \
  "s_cmp_gt_u32 s83, 5\n" \
  "s_cbranch_scc1 Lldnq_%=\n" \
  "s_add_u32 m0, s81, 18432\n" \
  "s_nop 0\n" \
  "global_load_lds_dwordx4 v5, s[76:77]\n" \
  "global_load_lds_dwordx4 v6, s[76:77] offset:1024\n" \
  "global_load_lds_dwordx4 v7, s[76:77] offset:2048\n" \
  "Lldnq_%=:\n" \
  "s_add_u32 s38, s80, 16384\n" \
  "s_add_u32 s39, s80, 103424\n" \
  "s_cmp_lt_u32 s80, 16384\n" \
  "s_cselect_b32 s38, s38, s39\n" \
  "s_sub_u32 s39, s80, 119808\n" \
  "s_cselect_b32 s80, s38, s39\n" \
  "s_mov_b32 m0, s80\n" \
  "s_and_b32 s36, s12, 15\n" \
  "s_add_u32 s36, s36, s85\n" \
  "s_add_u32 s37, s36, 9\n" \
  "s_min_u32 s36, s36, s37\n" \
  "s_lshl_b32 s37, s36, 7\n" \
  "s_add_u32 s72, s16, s37\n" \
  "s_addc_u32 s73, s17, 0\n" \
  "global_load_lds_dwordx4 v1, s[72:73]\n" \
  "global_load_lds_dwordx4 v2, s[72:73] offset:1024\n" \
  "global_load_lds_dwordx4 v3, s[72:73] offset:2048\n" \
  "global_load_lds_dwordx4 v4, s[72:73] offset:3072\n" \
  "s_mul_i32 s36, s36, 221184\n" \
  "s_lshr_b64 s[12:13], s[12:13], 4\n" \
  "s_add_u32 s76, s18, s36\n" \
  "s_addc_u32 s77, s19, 0\n" \
  "s_add_u32 s81, s81, 27648\n" \
  "s_cmp_ge_u32 s81, 119808\n" \
  "s_cselect_b32 s38, 82944, 0\n" \
  "s_sub_u32 s81, s81, s38\n" \
  "s_mov_b32 m0, s81\n" \
  "s_add_u32 s78, s76, 1152\n" \
  "s_addc_u32 s79, s77, 0\n" \
  "global_load_lds_dwordx4 v5, s[78:79]\n" \
  "global_load_lds_dwordx4 v6, s[78:79] offset:1024\n" \
  "global_load_lds_dwordx4 v7, s[78:79] offset:2048\n" \
  "s_bitcmp1_b32 s56, 0\n" \
  "s_cbranch_scc0 Lzf1_%=\n" \
  "s_load_dwordx2 s[92:93], s[52:53], 60\n" \
  "s_load_dwordx2 s[92:93], s[52:53], 1212\n" \
  "s_load_dwordx2 s[92:93], s[52:53], 2364\n" \
  "s_load_dwordx2 s[92:93], s[52:53], 3516\n" \
  "s_cmp_eq_u32 s83, 3\n" \
  "s_cselect_b32 s36, s55, s54\n" \
  "s_add_u32 s52, s52, s36\n" \
  "s_addc_u32 s53, s53, 0\n" \
  "Lzf1_%=:\n" \
  "s_add_u32 s83, s83, 1\n" \
  "s_waitcnt vmcnt(7)\n" \
  "s_barrier\n" \
  "s_cmp_lt_u32 s83, 8\n" \
  "s_cbranch_scc1 Lld_%=\n" \
  "s_add_u32 m0, s81, 9216\n" \
  "s_add_u32 s78, s76, 2304\n" \
  "s_addc_u32 s79, s77, 0\n" \
  "global_load_lds_dwordx4 v5, s[78:79]\n" \
  "global_load_lds_dwordx4 v6, s[78:79] offset:1024\n" \
  "global_load_lds_dwordx4 v7, s[78:79] offset:2048\n" \
  "s_mov_b32 s12, s74\n" \
  "s_add_u32 s82, s84, 55296\n" \
  "s_and_b32 s36, s12, 15\n" \
  "s_add_u32 s36, s36, s85\n" \
  "s_add_u32 s37, s36, 9\n" \
  "s_min_u32 s36, s36, s37\n" \
  "s_mul_i32 s36, s36, 221184\n" \
  "s_lshr_b64 s[12:13], s[12:13], 4\n" \
  "s_add_u32 s76, s18, s36\n" \
  "s_addc_u32 s77, s19, 0\n" \
  "s_mov_b32 m0, s82\n" \
  "s_nop 0\n" \
  "global_load_lds_dwordx4 v5, s[76:77]\n" \
  "global_load_lds_dwordx4 v6, s[76:77] offset:1024\n" \
  "global_load_lds_dwordx4 v7, s[76:77] offset:2048\n" \
  "s_and_b32 s36, s12, 15\n" \
  "s_add_u32 s36, s36, s85\n" \
  "s_add_u32 s37, s36, 9\n" \
  "s_min_u32 s36, s36, s37\n" \
  "s_mul_i32 s36, s36, 221184\n" \
  "s_lshr_b64 s[12:13], s[12:13], 4\n" \
  "s_add_u32 s76, s18, s36\n" \
  "s_addc_u32 s77, s19, 0\n" \
  "s_add_u32 m0, s82, 27648\n" \
  "s_nop 0\n" \
  "global_load_lds_dwordx4 v5, s[76:77]\n" \
  "global_load_lds_dwordx4 v6, s[76:77] offset:1024\n" \
  "global_load_lds_dwordx4 v7, s[76:77] offset:2048\n" \
  "s_waitcnt vmcnt(6)\n" \
  "s_barrier\n" \
  "s_and_b32 s36, s12, 15\n" \
  "s_add_u32 s36, s36, s85\n" \
  "s_add_u32 s37, s36, 9\n" \
  "s_min_u32 s36, s36, s37\n" \
  "s_mul_i32 s36, s36, 221184\n" \
  "s_lshr_b64 s[12:13], s[12:13], 4\n" \
  "s_add_u32 s76, s18, s36\n" \
  "s_addc_u32 s77, s19, 0\n" \
  "s_add_u32 m0, s82, 55296\n" \
  "s_nop 0\n" \
  "global_load_lds_dwordx4 v5, s[76:77]\n" \
  "global_load_lds_dwordx4 v6, s[76:77] offset:1024\n" \
  "global_load_lds_dwordx4 v7, s[76:77] offset:2048\n" \
  "s_branch Lldend_%=\n" \
    \
  "Lldx_%=:\n" \
  "v_and_b32 v14, 15, v124\n" \
  "v_lshlrev_b32 v14, 3, v14\n" \
  "s_lshl_b32 s36, s6, 9\n" \
  "s_mul_i32 s37, s7, 4224\n" \
  "s_add_u32 s36, s36, s37\n" \
  "v_lshl_add_u32 v15, v124, 7, s36\n" \
  "v_mov_b64 v[20:21], 0\n" \
  "v_mov_b64 v[22:23], 0\n" \
  "v_mov_b32 v12, v15\n" \
  "s_mov_b64 exec, 1\n" \
  "v_mov_b32 v18, 158848\n" \
  "ds_write_b128 v18, v[20:23]\n" \
  "ds_write_b128 v18, v[20:23] offset:16\n" \
  "ds_write_b128 v121, v[20:23] offset:12\n" \
  "ds_write_b128 v121, v[20:23] offset:28\n" \
  "v_mov_b32 v21, 0x80\n" \
  "v_mov_b32 v18, 158880\n" \
  "ds_write_b128 v18, v[20:23]\n" \
  "s_mov_b64 exec, -1\n" \
  "v_lshlrev_b32 v18, 4, v124\n" \
  "v_add_u32 v18, 156672, v18\n" \
  "v_lshrrev_b32 v19, 4, v124\n" \
  "v_and_b32 v15, 15, v124\n" \
  "v_lshlrev_b32 v15, 2, v15\n" \
  "v_sub_u32 v19, v19, v15\n" \
  "v_add_u32 v24, 28, v19\n" \
  "v_ashrrev_i32 v24, 31, v24\n" \
  "v_and_b32 v24, 0xff61b1e6, v24\n" \
  "v_xor_b32 v28, 0xff61b1e6, v24\n" \
  "v_add_u32 v25, 27, v19\n" \
  "v_ashrrev_i32 v25, 31, v25\n" \
  "v_and_b32 v25, 0xff61b1e6, v25\n" \
  "v_xor_b32 v29, 0xff61b1e6, v25\n" \
  "v_add_u32 v26, 26, v19\n" \
  "v_ashrrev_i32 v26, 31, v26\n" \
  "v_and_b32 v26, 0xff61b1e6, v26\n" \
  "v_xor_b32 v30, 0xff61b1e6, v26\n" \
  "v_add_u32 v27, 25, v19\n" \
  "v_ashrrev_i32 v27, 31, v27\n" \
  "v_and_b32 v27, 0xff61b1e6, v27\n" \
  "v_xor_b32 v31, 0xff61b1e6, v27\n" \
  "s_mul_i32 s41, s6, 112\n" \
  "s_sub_u32 s41, 896, s41\n" \
  "v_add_u32 v35, 0, v124\n" \
  "v_mul_u32_u24 v8, 7282, v35\n" \
  "v_lshrrev_b32 v8, 16, v8\n" \
  "v_mul_u32_u24 v9, 9, v8\n" \
  "v_sub_u32 v9, v35, v9\n" \
  "v_lshrrev_b32 v9, 3, v9\n" \
  "v_mul_u32_u24 v9, s41, v9\n" \
  "v_lshl_add_u32 v35, v35, 4, v9\n" \
  "v_mul_u32_u24 v9, 1008, v8\n" \
  "v_add_u32 v35, v35, v9\n" \
  "v_lshrrev_b32 v9, 4, v8\n" \
  "v_mul_u32_u24 v9, 4608, v9\n" \
  "v_add_u32 v35, v35, v9\n" \
  "v_and_b32 v9, 4, v8\n" \
  "v_mul_u32_u24 v9, 1152, v9\n" \
  "v_add_u32 v35, v35, v9\n" \
  "v_and_b32 v9, 8, v8\n" \
  "v_mul_u32_u24 v9, 576, v9\n" \
  "v_sub_u32 v35, v35, v9\n" \
  "v_add_u32 v36, 64, v124\n" \
  "v_mul_u32_u24 v8, 7282, v36\n" \
  "v_lshrrev_b32 v8, 16, v8\n" \
  "v_mul_u32_u24 v9, 9, v8\n" \
  "v_sub_u32 v9, v36, v9\n" \
  "v_lshrrev_b32 v9, 3, v9\n" \
  "v_mul_u32_u24 v9, s41, v9\n" \
  "v_lshl_add_u32 v36, v36, 4, v9\n" \
  "v_mul_u32_u24 v9, 1008, v8\n" \
  "v_add_u32 v36, v36, v9\n" \
  "v_lshrrev_b32 v9, 4, v8\n" \
  "v_mul_u32_u24 v9, 4608, v9\n" \
  "v_add_u32 v36, v36, v9\n" \
  "v_and_b32 v9, 4, v8\n" \
  "v_mul_u32_u24 v9, 1152, v9\n" \
  "v_add_u32 v36, v36, v9\n" \
  "v_and_b32 v9, 8, v8\n" \
  "v_mul_u32_u24 v9, 576, v9\n" \
  "v_sub_u32 v36, v36, v9\n" \
  "v_add_u32 v37, 128, v124\n" \
  "v_mul_u32_u24 v8, 7282, v37\n" \
  "v_lshrrev_b32 v8, 16, v8\n" \
  "v_mul_u32_u24 v9, 9, v8\n" \
  "v_sub_u32 v9, v37, v9\n" \
  "v_lshrrev_b32 v9, 3, v9\n" \
  "v_mul_u32_u24 v9, s41, v9\n" \
  "v_lshl_add_u32 v37, v37, 4, v9\n" \
  "v_mul_u32_u24 v9, 1008, v8\n" \
  "v_add_u32 v37, v37, v9\n" \
  "v_lshrrev_b32 v9, 4, v8\n" \
  "v_mul_u32_u24 v9, 4608, v9\n" \
  "v_add_u32 v37, v37, v9\n" \
  "v_and_b32 v9, 4, v8\n" \
  "v_mul_u32_u24 v9, 1152, v9\n" \
  "v_add_u32 v37, v37, v9\n" \
  "v_and_b32 v9, 8, v8\n" \
  "v_mul_u32_u24 v9, 576, v9\n" \
  "v_sub_u32 v37, v37, v9\n" \
  "v_add_u32 v38, 192, v124\n" \
  "v_mul_u32_u24 v8, 7282, v38\n" \
  "v_lshrrev_b32 v8, 16, v8\n" \
  "v_mul_u32_u24 v9, 9, v8\n" \
  "v_sub_u32 v9, v38, v9\n" \
  "v_lshrrev_b32 v9, 3, v9\n" \
  "v_mul_u32_u24 v9, s41, v9\n" \
  "v_lshl_add_u32 v38, v38, 4, v9\n" \
  "v_mul_u32_u24 v9, 1008, v8\n" \
  "v_add_u32 v38, v38, v9\n" \
  "v_lshrrev_b32 v9, 4, v8\n" \
  "v_mul_u32_u24 v9, 4608, v9\n" \
  "v_add_u32 v38, v38, v9\n" \
  "v_and_b32 v9, 4, v8\n" \
  "v_mul_u32_u24 v9, 1152, v9\n" \
  "v_add_u32 v38, v38, v9\n" \
  "v_and_b32 v9, 8, v8\n" \
  "v_mul_u32_u24 v9, 576, v9\n" \
  "v_sub_u32 v38, v38, v9\n" \
  "v_add_u32 v39, 256, v124\n" \
  "v_mul_u32_u24 v8, 7282, v39\n" \
  "v_lshrrev_b32 v8, 16, v8\n" \
  "v_mul_u32_u24 v9, 9, v8\n" \
  "v_sub_u32 v9, v39, v9\n" \
  "v_lshrrev_b32 v9, 3, v9\n" \
  "v_mul_u32_u24 v9, s41, v9\n" \
  "v_lshl_add_u32 v39, v39, 4, v9\n" \
  "v_mul_u32_u24 v9, 1008, v8\n" \
  "v_add_u32 v39, v39, v9\n" \
  "v_lshrrev_b32 v9, 4, v8\n" \
  "v_mul_u32_u24 v9, 4608, v9\n" \
  "v_add_u32 v39, v39, v9\n" \
  "v_and_b32 v9, 4, v8\n" \
  "v_mul_u32_u24 v9, 1152, v9\n" \
  "v_add_u32 v39, v39, v9\n" \
  "v_and_b32 v9, 8, v8\n" \
  "v_mul_u32_u24 v9, 576, v9\n" \
  "v_sub_u32 v39, v39, v9\n" \
  "v_subrev_u32 v36, 1024, v36\n" \
  "v_subrev_u32 v37, 2048, v37\n" \
  "v_subrev_u32 v38, 3072, v38\n" \
  "s_lshl_b32 s36, s6, 7\n" \
  "s_add_u32 s20, s20, s36\n" \
  "s_addc_u32 s21, s21, 0\n" \
  "s_mul_i32 s36, s7, 41472\n" \
  "s_add_u32 s78, s20, s36\n" \
  "s_addc_u32 s79, s21, 0\n" \
  "s_bfe_u32 s82, s74, 0x4000c\n" \
  "s_cmp_ge_u32 s7, 5\n" \
  "s_mov_b32 s36, 18432\n" \
  "s_cmov_b32 s36, 350048\n" \
  "s_cselect_b32 s38, 9, 4\n" \
  "s_lshl_b32 s37, s7, 5\n" \
  "s_add_u32 s36, s36, s37\n" \
  "s_cmp_eq_u32 s7, s38\n" \
  "s_cselect_b32 s37, 0, 16\n" \
  "s_cselect_b32 s41, s41, 0\n" \
  "s_add_u32 s36, s36, s41\n" \
  "v_and_b32 v8, 1, v124\n" \
  "v_mul_u32_u24 v34, s37, v8\n" \
  "v_lshrrev_b32 v8, 5, v124\n" \
  "v_mul_u32_u24 v8, 18, v8\n" \
  "v_bfe_u32 v9, v124, 4, 1\n" \
  "v_add_u32 v8, v8, v9\n" \
  "v_bfe_u32 v9, v124, 2, 2\n" \
  "v_mul_u32_u24 v9, 36, v9\n" \
  "v_add_u32 v8, v8, v9\n" \
  "v_bfe_u32 v9, v124, 1, 1\n" \
  "v_lshl_add_u32 v8, v8, 1, v9\n" \
  "v_mul_u32_u24 v8, 1152, v8\n" \
  "v_add3_u32 v34, v34, v8, s36\n" \
  "ds_write_b128 v18, v[24:27]\n" \
  "ds_write_b128 v18, v[28:31] offset:1024\n" \
  "s_mov_b32 s83, 0\n" \
  "s_waitcnt lgkmcnt(0)\n" \
  "s_getpc_b64 s[70:71]\n" \
  "Ltp_%=:\n" \
  "s_add_u32 s70, s70, Lt0_%=-Ltp_%=\n" \
  "s_addc_u32 s71, s71, 0\n" \
  "Lldx2_%=:\n" \
  "s_add_u32 s38, s80, 16384\n" \
  "s_add_u32 s39, s80, 103424\n" \
  "s_cmp_lt_u32 s80, 16384\n" \
  "s_cselect_b32 s38, s38, s39\n" \
  "s_sub_u32 s39, s80, 119808\n" \
  "s_cselect_b32 s80, s38, s39\n" \
  "s_mov_b32 m0, s80\n" \
  "s_and_b32 s36, s12, 15\n" \
  "s_add_u32 s36, s36, s85\n" \
  "s_add_u32 s37, s36, 9\n" \
  "s_min_u32 s36, s36, s37\n" \
  "s_lshl_b32 s37, s36, 7\n" \
  "s_add_u32 s72, s16, s37\n" \
  "s_addc_u32 s73, s17, 0\n" \
  "global_load_lds_dwordx4 v1, s[72:73]\n" \
  "global_load_lds_dwordx4 v2, s[72:73] offset:1024\n" \
  "global_load_lds_dwordx4 v3, s[72:73] offset:2048\n" \
  "global_load_lds_dwordx4 v4, s[72:73] offset:3072\n" \
  "s_swappc_b64 s[70:71], s[70:71]\n" \
  "s_waitcnt vmcnt(4)\n" \
  "s_cmp_lg_u32 s83, s82\n" \
  "s_cbranch_scc1 Lle1_%=\n" \
  "global_load_dwordx2 v[16:17], v14, s[24:25] sc1\n" \
  "s_mov_b64 exec, 15\n" \
  "global_atomic_add v12, v22, s[24:25] offset:128 sc1\n" \
  "s_mov_b64 exec, -1\n" \
  "Lle1_%=:\n" \
  "s_cmp_lg_u32 s83, 3\n" \
  "s_cbranch_scc1 Lle4_%=\n" \
  "s_load_dwordx2 s[92:93], s[24:25], 60\n" \
  "Lle4_%=:\n" \
  "s_bitcmp1_b32 s56, 0\n" \
  "s_cbranch_scc0 Lzf2_%=\n" \
  "s_load_dwordx2 s[92:93], s[52:53], 60\n" \
  "s_load_dwordx2 s[92:93], s[52:53], 1212\n" \
  "s_load_dwordx2 s[92:93], s[52:53], 2364\n" \
  "s_load_dwordx2 s[92:93], s[52:53], 3516\n" \
  "s_cmp_eq_u32 s83, 3\n" \
  "s_cselect_b32 s36, s55, s54\n" \
  "s_add_u32 s52, s52, s36\n" \
  "s_addc_u32 s53, s53, 0\n" \
  "Lzf2_%=:\n" \
  "s_lshr_b64 s[12:13], s[12:13], 4\n" \
  "s_add_u32 s83, s83, 1\n" \
  "s_cmp_lt_u32 s83, 8\n" \
  "s_barrier\n" \
  "s_cbranch_scc1 Lldx2_%=\n" \
  "s_waitcnt vmcnt(0)\n" \
  "v_lshl_add_u64 v[16:17], v[16:17], 0, 1\n" \
  "v_add_u32 v14, 158720, v14\n" \
  "ds_write_b64 v14, v[16:17]\n" \
  "s_waitcnt vmcnt(0) lgkmcnt(0)\n" \
  "s_barrier\n" \
  "v_mov_b32 v8, 17\n" \
    \
  "Lldend_%=:\n" \
  "s_waitcnt vmcnt(0)\n" \
  "s_mov_b64 exec, 1\n" \
  "ds_add_u32 v121, v122 offset:24\n" \
  "s_mov_b64 exec, -1\n" \
  "s_setprio 0\n" \
  "s_getpc_b64 s[70:71]\n" \
  "s_branch Lsetup_%=\n" \
  "ds_read_b128 v[64:67], v123\n" \
  "ds_read_b128 v[68:71], v123 offset:32\n" \
  "ds_read_b128 v[72:75], v123 offset:64\n" \
  "ds_read_b128 v[76:79], v123 offset:96\n" \
  "s_cmp_lg_u32 s4, 3\n" \
  "s_cbranch_scc1 Lle3_%=\n" \
  "s_mov_b64 exec, 1\n" \
  "ds_add_u32 v121, v122 offset:40\n" \
  "s_mov_b64 exec, -1\n" \
  "s_sleep 16\n" \
  "s_setprio 3\n" \
  "s_mov_b32 m0, 163072\n" \
  "s_mov_b32 exec_hi, 0\n" \
  "s_cmp_ge_u32 s7, 10\n" \
  "global_load_lds_dwordx4 v39, s[78:79]\n" \
  "s_mov_b32 m0, 158976\n" \
  "s_mov_b32 exec_hi, -1\n" \
  "global_load_lds_dwordx4 v35, s[78:79]\n" \
  "global_load_lds_dwordx4 v36, s[78:79] offset:1024\n" \
  "global_load_lds_dwordx4 v37, s[78:79] offset:2048\n" \
  "global_load_lds_dwordx4 v38, s[78:79] offset:3072\n" \
  "s_mov_b32 m0, 154624\n" \
  "s_cbranch_scc1 Lzr3_%=\n" \
  "global_load_lds_dwordx4 v34, s[20:21]\n" \
  "Lzr3_%=:\n" \
  "s_waitcnt vmcnt(0)\n" \
  "v_mov_b32 v8, 64\n" \
  "s_mov_b64 exec, 1\n" \
  "ds_add_u32 v121, v8 offset:24\n" \
  "s_mov_b64 exec, -1\n" \
  "s_setprio 0\n" \
  "s_branch Lattn2_%=\n" \
  "Lle3_%=:\n" \
  "s_sleep 14\n" \
  "s_branch Lattn_%=\n" \
    \
  "Lsetup_%=:\n" \
  "v_and_b32 v64, 31, v124\n" \
  "v_lshrrev_b32 v65, 5, v124\n" \
  "v_and_b32 v66, 3, v124\n" \
  "v_lshl_add_u32 v66, v66, 4, v65\n" \
  "v_bfe_u32 v67, v124, 2, 3\n" \
  "v_sub_u32 v66, v66, v67\n" \
  "v_lshlrev_b32 v66, 4, v66\n" \
  "v_add_u32 v123, 156784, v66\n" \
  "s_mul_i32 s36, s10, 9216\n" \
  "s_lshr_b32 s37, s4, 1\n" \
  "s_lshl_b32 s37, s37, 3\n" \
  "s_lshl_b32 s38, s4, 10\n" \
  "s_add_u32 s39, s36, s38\n" \
  "v_lshl_add_u32 v114, v124, 4, s39\n" \
  "v_add_u32 v106, 136192, v114\n" \
  "s_add_u32 s39, s36, s37\n" \
  "s_add_u32 s39, s39, 4096\n" \
  "v_lshl_add_u32 v115, v64, 4, s39\n" \
  "v_add_u32 v107, 136192, v115\n" \
  "s_bitcmp1_b32 s4, 0\n" \
  "s_cselect_b32 s39, 0, -1\n" \
  "s_not_b32 s40, s39\n" \
  "v_mov_b32 v108, s39\n" \
  "v_mov_b32 v109, s40\n" \
  "s_mov_b32 exec_lo, 0\n" \
  "v_mov_b64 v[108:109], 0\n" \
  "s_mov_b32 exec_lo, -1\n" \
  "v_mov_b32 v116, 0x3f80\n" \
  "v_mov_b32 v117, 0\n" \
  "v_mov_b64 v[118:119], v[116:117]\n" \
  "s_sub_u32 s36, 73728, s36\n" \
  "v_lshl_add_u32 v68, v65, 4, v64\n" \
  "s_add_u32 s39, s36, s38\n" \
  "v_lshl_add_u32 v96, v68, 4, s39\n" \
  "v_add_u32 v97, 512, v96\n" \
  "v_lshl_add_u32 v68, v65, 3, v64\n" \
  "s_lshl_b32 s39, s37, 3\n" \
  "s_add_u32 s39, s39, s36\n" \
  "s_add_u32 s39, s39, 3840\n" \
  "s_mov_b32 exec_lo, 0x000f0000\n" \
  "s_mov_b32 exec_hi, 0x000f0000\n" \
  "v_lshl_add_u32 v96, v68, 4, s39\n" \
  "v_add_u32 v97, 256, v96\n" \
  "s_mov_b64 exec, -1\n" \
  "s_mov_b32 s86, 0x000fffff\n" \
  "s_mov_b32 s87, 0x000fffff\n" \
  "v_mov_b32 v121, 158884\n" \
  "v_mov_b32 v122, 1\n" \
  "s_lshl_b32 s61, s7, 1\n" \
  "s_add_u32 s61, s61, s10\n" \
  "s_lshl_b32 s61, s61, 1\n" \
  "s_add_u32 s61, s61, 2\n" \
  "s_min_u32 s61, s61, 10\n" \
  "s_cmp_eq_u32 s10, 0\n" \
  "s_mov_b32 s60, 0x120a0207\n" \
  "s_cmov_b32 s60, 0x1a120a02\n" \
  "s_cselect_b64 s[62:63], 1, 0\n" \
  "s_cselect_b64 s[64:65], 0, 1\n" \
  "s_lshl_b32 s39, s61, 2\n" \
  "s_sub_u32 s39, s39, 8\n" \
  "s_lshl_b64 s[40:41], -1, s39\n" \
  "s_andn2_b32 s60, s60, s40\n" \
  "s_and_b32 s40, s40, 0x06060606\n" \
  "s_or_b32 s60, s60, s40\n" \
  "s_lshl_b32 s36, s6, 2\n" \
  "s_add_u32 s36, s36, s4\n" \
  "s_lshl_b32 s37, s7, 1\n" \
  "s_add_u32 s37, s37, s10\n" \
  "s_mul_i32 s37, s37, 73856\n" \
  "s_lshl_b32 s38, s36, 9\n" \
  "s_add_u32 s38, s38, s37\n" \
  "s_lshl_b32 s36, s36, 6\n" \
  "s_add_u32 s36, s36, s37\n" \
  "s_add_u32 s36, s36, 16384\n" \
  "v_bfe_u32 v66, v124, 4, 1\n" \
  "v_mul_u32_u24 v66, 18432, v66\n" \
  "v_and_b32 v67, 15, v124\n" \
  "v_lshl_add_u32 v68, v65, 4, v67\n" \
  "v_lshl_add_u32 v68, v68, 4, v66\n" \
  "v_add_u32 v125, s38, v68\n" \
  "v_lshl_add_u32 v68, v67, 2, v66\n" \
  "v_add_u32 v126, s36, v68\n" \
  "s_add_u32 s70, s70, 4\n" \
  "s_addc_u32 s71, s71, 0\n" \
  "s_setpc_b64 s[70:71]\n" \
    \
  "Lcomp_%=:\n" \
  "v_mov_b64 v[0:1], 0\n" \
  "v_mov_b64 v[2:3], 0\n" \
  "v_mov_b64 v[4:5], 0\n" \
  "v_mov_b64 v[6:7], 0\n" \
  "v_mov_b64 v[8:9], 0\n" \
  "v_mov_b64 v[10:11], 0\n" \
  "v_mov_b64 v[12:13], 0\n" \
  "v_mov_b64 v[14:15], 0\n" \
  "v_mov_b64 v[16:17], 0\n" \
  "v_mov_b64 v[18:19], 0\n" \
  "v_mov_b64 v[20:21], 0\n" \
  "v_mov_b64 v[22:23], 0\n" \
  "v_mov_b64 v[24:25], 0\n" \
  "v_mov_b64 v[26:27], 0\n" \
  "v_mov_b64 v[28:29], 0\n" \
  "v_mov_b64 v[30:31], 0\n" \
  "v_mov_b64 v[32:33], 0\n" \
  "v_mov_b64 v[34:35], 0\n" \
  "v_mov_b64 v[36:37], 0\n" \
  "v_mov_b64 v[38:39], 0\n" \
  "v_and_b32 v40, 15, v124\n" \
  "v_lshrrev_b32 v41, 4, v124\n" \
  "v_and_b32 v42, 7, v124\n" \
  "v_xor_b32 v42, v42, v41\n" \
  "s_lshl_b32 s36, s4, 12\n" \
  "v_lshl_add_u32 v62, v40, 7, s36\n" \
  "v_lshl_add_u32 v62, v42, 4, v62\n" \
  "v_xor_b32 v63, 64, v62\n" \
  "v_add_u32 v98, 119808, v62\n" \
  "v_add_u32 v99, 119808, v63\n" \
  "v_lshrrev_b32 v42, 2, v124\n" \
  "v_mul_u32_u24 v42, 288, v42\n" \
  "v_and_b32 v43, 3, v124\n" \
  "s_mul_i32 s36, s5, 9216\n" \
  "s_add_u32 s36, s36, 27648\n" \
  "s_cmp_eq_u32 s5, 3\n" \
  "s_cselect_b32 s72, 0x1f, 0xff\n" \
  "s_cselect_b32 s37, 0, 4608\n" \
  "v_add_u32 v42, s36, v42\n" \
  "v_lshl_add_u32 v60, v43, 3, v42\n" \
  "v_and_b32 v44, 1, v124\n" \
  "v_mul_u32_u24 v44, 72, v44\n" \
  "v_and_b32 v43, 2, v124\n" \
  "v_mul_u32_u24 v43, s37, v43\n" \
  "v_add3_u32 v61, v42, v43, v44\n" \
  "s_mov_b32 s73, 3\n" \
  "s_cbranch_scc1 Lg5_%=\n" \
  "s_mul_i32 s36, s4, 4608\n" \
  "s_sub_u32 s37, 32256, s36\n" \
  "v_and_b32 v43, 31, v124\n" \
  "v_lshlrev_b32 v43, 4, v43\n" \
  "s_add_u32 s38, s36, 4096\n" \
  "v_add_u32 v104, s38, v43\n" \
  "v_add_u32 v105, 36864, v104\n" \
  "s_add_u32 s38, s37, 50176\n" \
  "s_mov_b32 exec_lo, 0\n" \
  "v_add_u32 v105, s38, v43\n" \
  "s_mov_b32 exec_lo, -1\n" \
  "v_lshrrev_b32 v43, 5, v124\n" \
  "v_bfe_u32 v44, v124, 2, 3\n" \
  "v_lshl_add_u32 v43, v43, 4, v44\n" \
  "v_and_b32 v44, 1, v124\n" \
  "v_lshl_add_u32 v43, v43, 1, v44\n" \
  "v_lshl_add_u32 v100, v43, 3, s38\n" \
  "v_and_b32 v44, 7, v124\n" \
  "v_lshl_add_u32 v43, v41, 3, v44\n" \
  "s_add_u32 s38, s37, 4096\n" \
  "v_lshl_add_u32 v101, v43, 4, s38\n" \
  "s_cmp_eq_u32 s5, 2\n" \
  "s_cselect_b32 s36, s37, s36\n" \
  "s_mov_b32 s39, 36864\n" \
  "s_cmov_b32 s39, 46080\n" \
  "v_lshl_add_u32 v103, v124, 4, s36\n" \
  "v_add_u32 v102, s39, v103\n" \
  "s_lshl_b32 s37, s7, 3\n" \
  "s_add_u32 s37, s37, s6\n" \
  "s_mul_i32 s37, s37, 36992\n" \
  "s_waitcnt lgkmcnt(0)\n" \
  "s_add_u32 s26, s26, s37\n" \
  "s_addc_u32 s27, s27, 0\n" \
  "v_mov_b32 v121, 158848\n" \
  "v_mov_b32 v64, 1\n" \
  "v_mov_b32 v125, -1\n" \
  "v_mov_b64 v[76:77], 0\n" \
  "v_and_b32 v66, 15, v124\n" \
  "v_lshlrev_b32 v65, 3, v66\n" \
  "s_lshl_b32 s36, s6, 9\n" \
  "s_mul_i32 s37, s7, 4224\n" \
  "s_add_u32 s37, s37, s36\n" \
  "v_add_u32 v107, s37, v65\n" \
  "v_add_u32 v65, 158720, v65\n" \
  "s_lshl_b32 s37, s7, 3\n" \
  "s_add_u32 s38, s36, s37\n" \
  "s_sub_u32 s36, s36, s37\n" \
  "v_mul_u32_u24 v67, 4224, v66\n" \
  "v_mul_u32_u24 v66, 4232, v66\n" \
  "v_add_u32 v67, s38, v67\n" \
  "v_add_u32 v66, s36, v66\n" \
  "s_mul_i32 s36, s6, 83072\n" \
  "s_add_u32 s36, s36, 8388608\n" \
  "s_add_u32 s62, s28, s36\n" \
  "s_addc_u32 s63, s29, 0\n" \
  "v_lshrrev_b32 v41, 4, v124\n" \
  "v_bfe_u32 v42, v124, 2, 2\n" \
  "v_lshl_add_u32 v41, v41, 3, v42\n" \
  "v_and_b32 v42, 3, v124\n" \
  "v_lshlrev_b32 v42, 3, v42\n" \
  "v_lshl_add_u32 v71, v41, 5, v42\n" \
  "v_add_u32 v71, 154624, v71\n" \
  "v_mul_u32_u24 v41, 144, v41\n" \
  "s_lshl_b32 s36, s4, 5\n" \
  "s_add_u32 s36, s36, 158976\n" \
  "v_add3_u32 v70, v41, v42, s36\n" \
  "s_lshl_b32 s37, s7, 1\n" \
  "s_add_u32 s37, s37, s10\n" \
  "s_mul_i32 s37, s37, 73856\n" \
  "s_lshl_b32 s38, s10, 2\n" \
  "s_add_u32 s60, s38, s4\n" \
  "s_bitcmp1_b32 200, s60\n" \
  "s_cselect_b32 s39, 17, 1\n" \
  "s_cselect_b32 s74, 32, 0x80\n" \
  "s_cmp_eq_u32 s7, 15\n" \
  "s_cmov_b32 s74, 0x80\n" \
  "v_mov_b32 v72, s39\n" \
  "s_lshl_b32 s61, s6, 0\n" \
  "s_and_b32 s61, s61, 0\n" \
  "s_mul_i32 s39, s4, 18432\n" \
  "s_add_u32 s37, s37, s39\n" \
  "s_add_u32 s28, s28, s37\n" \
  "s_addc_u32 s29, s29, 0\n" \
  "s_xor_b32 s37, s61, 0\n" \
  "s_lshl_b32 s37, s37, 12\n" \
  "s_add_u32 s64, s28, s37\n" \
  "s_addc_u32 s65, s29, 0\n" \
  "s_xor_b32 s37, s61, 1\n" \
  "s_lshl_b32 s37, s37, 12\n" \
  "s_add_u32 s66, s28, s37\n" \
  "s_addc_u32 s67, s29, 0\n" \
  "s_xor_b32 s37, s61, 2\n" \
  "s_lshl_b32 s37, s37, 12\n" \
  "s_add_u32 s68, s28, s37\n" \
  "s_addc_u32 s69, s29, 0\n" \
  "s_xor_b32 s37, s61, 3\n" \
  "s_lshl_b32 s37, s37, 12\n" \
  "s_add_u32 s70, s28, s37\n" \
  "s_addc_u32 s71, s29, 0\n" \
  "s_add_u32 s28, s28, 16384\n" \
  "s_addc_u32 s29, s29, 0\n" \
  "v_lshlrev_b32 v112, 4, v124\n" \
  "v_add_u32 v78, 4096, v112\n" \
  "v_lshlrev_b32 v126, 2, v124\n" \
  "v_add_u32 v113, 36864, v112\n" \
  "v_add_u32 v114, 119808, v112\n" \
  "v_lshrrev_b32 v41, 4, v124\n" \
  "v_and_b32 v42, 7, v124\n" \
  "v_lshl_add_u32 v41, v41, 3, v42\n" \
  "v_lshlrev_b32 v41, 4, v41\n" \
  "v_add_u32 v122, 40960, v41\n" \
  "v_add_u32 v123, 123904, v41\n" \
  "s_mul_i32 s36, s60, 2560\n" \
  "v_and_b32 v41, 15, v124\n" \
  "v_mul_u32_u24 v41, 144, v41\n" \
  "v_lshrrev_b32 v42, 4, v124\n" \
  "v_lshl_add_u32 v41, v42, 3, v41\n" \
  "v_add_u32 v115, s36, v41\n" \
  "v_lshrrev_b32 v41, 3, v124\n" \
  "v_lshl_add_u32 v42, v41, 4, v112\n" \
  "v_add_u32 v116, s36, v42\n" \
  "v_mul_u32_u24 v41, 2176, v41\n" \
  "v_add_u32 v117, v41, v112\n" \
  "v_add_u32 v118, 18432, v117\n" \
  "s_mul_i32 s37, s6, 112\n" \
  "s_sub_u32 s37, 1024, s37\n" \
  "v_mul_u32_u24 v41, 2304, v124\n" \
  "v_add_u32 v119, s37, v41\n" \
  "s_lshl_b32 s36, s7, 3\n" \
  "s_add_u32 s36, s36, s60\n" \
  "s_mul_i32 s36, s36, 36864\n" \
  "s_lshl_b32 s37, s6, 7\n" \
  "s_add_u32 s36, s36, s37\n" \
  "s_add_u32 s22, s22, s36\n" \
  "s_addc_u32 s23, s23, 0\n" \
  "s_setprio 2\n" \
  "s_cmp_eq_u32 s5, 2\n" \
  "s_cbranch_scc1 Lv0_%=\n" \
  "Lg5_%=:\n" \
  "s_waitcnt lgkmcnt(0)\n" \
  "s_barrier\n" \
  "ds_read_b64_tr_b16 v[80:81], v60\n" \
  "ds_read_b128 v[40:43], v62\n" \
  "ds_read_b64_tr_b16 v[82:83], v60 offset:144\n" \
  "ds_read_b64_tr_b16 v[84:85], v60 offset:40\n" \
  "ds_read_b128 v[44:47], v62 offset:2048\n" \
  "ds_read_b64_tr_b16 v[86:87], v60 offset:184\n" \
  "ds_read_b64_tr_b16 v[88:89], v60 offset:72\n" \
  "ds_read_b64_tr_b16 v[90:91], v60 offset:216\n" \
    \
  "Lg5l_%=:\n" \
  "s_waitcnt lgkmcnt(5)\n" \
  "v_mfma_f32_16x16x32_bf16 v[0:3], v[80:83], v[40:43], v[0:3]\n" \
  "ds_read_b64_tr_b16 v[92:93], v60 offset:112\n" \
  "ds_read_b128 v[48:51], v63\n" \
  "s_waitcnt lgkmcnt(5)\n" \
  "v_mfma_f32_16x16x32_bf16 v[20:23], v[80:83], v[44:47], v[20:23]\n" \
  "ds_read_b64_tr_b16 v[94:95], v60 offset:256\n" \
  "s_waitcnt lgkmcnt(5)\n" \
  "v_mfma_f32_16x16x32_bf16 v[4:7], v[84:87], v[40:43], v[4:7]\n" \
  "ds_read_b64_tr_b16 v[56:57], v61 offset:32\n" \
  "ds_read_b128 v[52:55], v63 offset:2048\n" \
  "v_mfma_f32_16x16x32_bf16 v[24:27], v[84:87], v[44:47], v[24:27]\n" \
  "ds_read_b64_tr_b16 v[58:59], v61 offset:176\n" \
  "s_waitcnt lgkmcnt(6)\n" \
  "v_mfma_f32_16x16x32_bf16 v[8:11], v[88:91], v[40:43], v[8:11]\n" \
  "ds_read_b64_tr_b16 v[80:81], v60 offset:4608\n" \
  "v_mfma_f32_16x16x32_bf16 v[28:31], v[88:91], v[44:47], v[28:31]\n" \
  "ds_read_b64_tr_b16 v[82:83], v60 offset:4752\n" \
  "s_waitcnt lgkmcnt(5)\n" \
  "v_mfma_f32_16x16x32_bf16 v[12:15], v[92:95], v[40:43], v[12:15]\n" \
  "ds_read_b64_tr_b16 v[84:85], v60 offset:4648\n" \
  "v_mfma_f32_16x16x32_bf16 v[32:35], v[92:95], v[44:47], v[32:35]\n" \
  "ds_read_b64_tr_b16 v[86:87], v60 offset:4792\n" \
  "s_waitcnt lgkmcnt(4)\n" \
  "v_mfma_f32_16x16x32_bf16 v[16:19], v[56:59], v[40:43], v[16:19]\n" \
  "ds_read_b64_tr_b16 v[88:89], v60 offset:4680\n" \
  "v_mfma_f32_16x16x32_bf16 v[36:39], v[56:59], v[44:47], v[36:39]\n" \
  "ds_read_b64_tr_b16 v[90:91], v60 offset:4824\n" \
  "s_waitcnt lgkmcnt(4)\n" \
  "v_mfma_f32_16x16x32_bf16 v[0:3], v[80:83], v[48:51], v[0:3]\n" \
  "ds_read_b64_tr_b16 v[92:93], v60 offset:4720\n" \
  "v_mfma_f32_16x16x32_bf16 v[20:23], v[80:83], v[52:55], v[20:23]\n" \
  "ds_read_b64_tr_b16 v[94:95], v60 offset:4864\n" \
  "s_waitcnt lgkmcnt(4)\n" \
  "v_mfma_f32_16x16x32_bf16 v[4:7], v[84:87], v[48:51], v[4:7]\n" \
  "ds_read_b64_tr_b16 v[56:57], v61 offset:4640\n" \
  "s_bitcmp1_b32 s72, 0\n" \
  "v_mfma_f32_16x16x32_bf16 v[24:27], v[84:87], v[52:55], v[24:27]\n" \
  "ds_read_b64_tr_b16 v[58:59], v61 offset:4784\n" \
  "s_cbranch_scc0 Lg5b0_%=\n" \
  "s_waitcnt lgkmcnt(0)\n" \
  "s_barrier\n" \
  "Lg5b0_%=:\n" \
  "s_waitcnt lgkmcnt(4)\n" \
  "v_mfma_f32_16x16x32_bf16 v[8:11], v[88:91], v[48:51], v[8:11]\n" \
  "ds_read_b64_tr_b16 v[80:81], v60 offset:27648\n" \
  "ds_read_b128 v[40:43], v62 offset:16384\n" \
  "v_mfma_f32_16x16x32_bf16 v[28:31], v[88:91], v[52:55], v[28:31]\n" \
  "ds_read_b64_tr_b16 v[82:83], v60 offset:27792\n" \
  "s_waitcnt lgkmcnt(5)\n" \
  "v_mfma_f32_16x16x32_bf16 v[12:15], v[92:95], v[48:51], v[12:15]\n" \
  "ds_read_b64_tr_b16 v[84:85], v60 offset:27688\n" \
  "ds_read_b128 v[44:47], v62 offset:18432\n" \
  "v_mfma_f32_16x16x32_bf16 v[32:35], v[92:95], v[52:55], v[32:35]\n" \
  "ds_read_b64_tr_b16 v[86:87], v60 offset:27832\n" \
  "s_waitcnt lgkmcnt(6)\n" \
  "v_mfma_f32_16x16x32_bf16 v[16:19], v[56:59], v[48:51], v[16:19]\n" \
  "ds_read_b64_tr_b16 v[88:89], v60 offset:27720\n" \
  "v_mfma_f32_16x16x32_bf16 v[36:39], v[56:59], v[52:55], v[36:39]\n" \
  "ds_read_b64_tr_b16 v[90:91], v60 offset:27864\n" \
  "s_waitcnt lgkmcnt(5)\n" \
  "v_mfma_f32_16x16x32_bf16 v[0:3], v[80:83], v[40:43], v[0:3]\n" \
  "ds_read_b64_tr_b16 v[92:93], v60 offset:27760\n" \
  "ds_read_b128 v[48:51], v63 offset:16384\n" \
  "s_waitcnt lgkmcnt(5)\n" \
  "v_mfma_f32_16x16x32_bf16 v[20:23], v[80:83], v[44:47], v[20:23]\n" \
  "ds_read_b64_tr_b16 v[94:95], v60 offset:27904\n" \
  "s_waitcnt lgkmcnt(5)\n" \
  "v_mfma_f32_16x16x32_bf16 v[4:7], v[84:87], v[40:43], v[4:7]\n" \
  "ds_read_b64_tr_b16 v[56:57], v61 offset:27680\n" \
  "ds_read_b128 v[52:55], v63 offset:18432\n" \
  "v_mfma_f32_16x16x32_bf16 v[24:27], v[84:87], v[44:47], v[24:27]\n" \
  "ds_read_b64_tr_b16 v[58:59], v61 offset:27824\n" \
  "s_waitcnt lgkmcnt(6)\n" \
  "v_mfma_f32_16x16x32_bf16 v[8:11], v[88:91], v[40:43], v[8:11]\n" \
  "ds_read_b64_tr_b16 v[80:81], v60 offset:32256\n" \
  "v_mfma_f32_16x16x32_bf16 v[28:31], v[88:91], v[44:47], v[28:31]\n" \
  "ds_read_b64_tr_b16 v[82:83], v60 offset:32400\n" \
  "s_waitcnt lgkmcnt(5)\n" \
  "v_mfma_f32_16x16x32_bf16 v[12:15], v[92:95], v[40:43], v[12:15]\n" \
  "ds_read_b64_tr_b16 v[84:85], v60 offset:32296\n" \
  "v_mfma_f32_16x16x32_bf16 v[32:35], v[92:95], v[44:47], v[32:35]\n" \
  "ds_read_b64_tr_b16 v[86:87], v60 offset:32440\n" \
  "s_waitcnt lgkmcnt(4)\n" \
  "v_mfma_f32_16x16x32_bf16 v[16:19], v[56:59], v[40:43], v[16:19]\n" \
  "ds_read_b64_tr_b16 v[88:89], v60 offset:32328\n" \
  "v_mfma_f32_16x16x32_bf16 v[36:39], v[56:59], v[44:47], v[36:39]\n" \
  "ds_read_b64_tr_b16 v[90:91], v60 offset:32472\n" \
  "s_waitcnt lgkmcnt(4)\n" \
  "v_mfma_f32_16x16x32_bf16 v[0:3], v[80:83], v[48:51], v[0:3]\n" \
  "ds_read_b64_tr_b16 v[92:93], v60 offset:32368\n" \
  "v_mfma_f32_16x16x32_bf16 v[20:23], v[80:83], v[52:55], v[20:23]\n" \
  "ds_read_b64_tr_b16 v[94:95], v60 offset:32512\n" \
  "s_waitcnt lgkmcnt(4)\n" \
  "v_mfma_f32_16x16x32_bf16 v[4:7], v[84:87], v[48:51], v[4:7]\n" \
  "ds_read_b64_tr_b16 v[56:57], v61 offset:32288\n" \
  "s_bitcmp1_b32 s72, 1\n" \
  "v_mfma_f32_16x16x32_bf16 v[24:27], v[84:87], v[52:55], v[24:27]\n" \
  "ds_read_b64_tr_b16 v[58:59], v61 offset:32432\n" \
  "s_cbranch_scc0 Lg5b1_%=\n" \
  "s_waitcnt lgkmcnt(0)\n" \
  "s_barrier\n" \
  "Lg5b1_%=:\n" \
  "s_waitcnt lgkmcnt(4)\n" \
  "v_mfma_f32_16x16x32_bf16 v[8:11], v[88:91], v[48:51], v[8:11]\n" \
  "ds_read_b64_tr_b16 v[80:81], v60 offset:55296\n" \
  "ds_read_b128 v[40:43], v98\n" \
  "v_mfma_f32_16x16x32_bf16 v[28:31], v[88:91], v[52:55], v[28:31]\n" \
  "ds_read_b64_tr_b16 v[82:83], v60 offset:55440\n" \
  "s_waitcnt lgkmcnt(5)\n" \
  "v_mfma_f32_16x16x32_bf16 v[12:15], v[92:95], v[48:51], v[12:15]\n" \
  "ds_read_b64_tr_b16 v[84:85], v60 offset:55336\n" \
  "ds_read_b128 v[44:47], v98 offset:2048\n" \
  "v_mfma_f32_16x16x32_bf16 v[32:35], v[92:95], v[52:55], v[32:35]\n" \
  "ds_read_b64_tr_b16 v[86:87], v60 offset:55480\n" \
  "s_waitcnt lgkmcnt(6)\n" \
  "v_mfma_f32_16x16x32_bf16 v[16:19], v[56:59], v[48:51], v[16:19]\n" \
  "ds_read_b64_tr_b16 v[88:89], v60 offset:55368\n" \
  "v_mfma_f32_16x16x32_bf16 v[36:39], v[56:59], v[52:55], v[36:39]\n" \
  "ds_read_b64_tr_b16 v[90:91], v60 offset:55512\n" \
  "s_waitcnt lgkmcnt(5)\n" \
  "v_mfma_f32_16x16x32_bf16 v[0:3], v[80:83], v[40:43], v[0:3]\n" \
  "ds_read_b64_tr_b16 v[92:93], v60 offset:55408\n" \
  "ds_read_b128 v[48:51], v99\n" \
  "s_waitcnt lgkmcnt(5)\n" \
  "v_mfma_f32_16x16x32_bf16 v[20:23], v[80:83], v[44:47], v[20:23]\n" \
  "ds_read_b64_tr_b16 v[94:95], v60 offset:55552\n" \
  "s_waitcnt lgkmcnt(5)\n" \
  "v_mfma_f32_16x16x32_bf16 v[4:7], v[84:87], v[40:43], v[4:7]\n" \
  "ds_read_b64_tr_b16 v[56:57], v61 offset:55328\n" \
  "ds_read_b128 v[52:55], v99 offset:2048\n" \
  "v_mfma_f32_16x16x32_bf16 v[24:27], v[84:87], v[44:47], v[24:27]\n" \
  "ds_read_b64_tr_b16 v[58:59], v61 offset:55472\n" \
  "s_waitcnt lgkmcnt(6)\n" \
  "v_mfma_f32_16x16x32_bf16 v[8:11], v[88:91], v[40:43], v[8:11]\n" \
  "ds_read_b64_tr_b16 v[80:81], v60 offset:59904\n" \
  "v_mfma_f32_16x16x32_bf16 v[28:31], v[88:91], v[44:47], v[28:31]\n" \
  "ds_read_b64_tr_b16 v[82:83], v60 offset:60048\n" \
  "s_waitcnt lgkmcnt(5)\n" \
  "v_mfma_f32_16x16x32_bf16 v[12:15], v[92:95], v[40:43], v[12:15]\n" \
  "ds_read_b64_tr_b16 v[84:85], v60 offset:59944\n" \
  "v_mfma_f32_16x16x32_bf16 v[32:35], v[92:95], v[44:47], v[32:35]\n" \
  "ds_read_b64_tr_b16 v[86:87], v60 offset:60088\n" \
  "s_waitcnt lgkmcnt(4)\n" \
  "v_mfma_f32_16x16x32_bf16 v[16:19], v[56:59], v[40:43], v[16:19]\n" \
  "ds_read_b64_tr_b16 v[88:89], v60 offset:59976\n" \
  "v_mfma_f32_16x16x32_bf16 v[36:39], v[56:59], v[44:47], v[36:39]\n" \
  "ds_read_b64_tr_b16 v[90:91], v60 offset:60120\n" \
  "s_waitcnt lgkmcnt(4)\n" \
  "v_mfma_f32_16x16x32_bf16 v[0:3], v[80:83], v[48:51], v[0:3]\n" \
  "ds_read_b64_tr_b16 v[92:93], v60 offset:60016\n" \
  "v_mfma_f32_16x16x32_bf16 v[20:23], v[80:83], v[52:55], v[20:23]\n" \
  "ds_read_b64_tr_b16 v[94:95], v60 offset:60160\n" \
  "s_waitcnt lgkmcnt(4)\n" \
  "v_mfma_f32_16x16x32_bf16 v[4:7], v[84:87], v[48:51], v[4:7]\n" \
  "ds_read_b64_tr_b16 v[56:57], v61 offset:59936\n" \
  "s_bitcmp1_b32 s72, 2\n" \
  "v_mfma_f32_16x16x32_bf16 v[24:27], v[84:87], v[52:55], v[24:27]\n" \
  "ds_read_b64_tr_b16 v[58:59], v61 offset:60080\n" \
  "s_cbranch_scc0 Lg5n_%=\n" \
  "s_waitcnt lgkmcnt(0)\n" \
  "s_barrier\n" \
  "Lg5b2_%=:\n" \
  "s_waitcnt lgkmcnt(4)\n" \
  "v_mfma_f32_16x16x32_bf16 v[8:11], v[88:91], v[48:51], v[8:11]\n" \
  "ds_read_b64_tr_b16 v[80:81], v60\n" \
  "ds_read_b128 v[40:43], v62\n" \
  "v_mfma_f32_16x16x32_bf16 v[28:31], v[88:91], v[52:55], v[28:31]\n" \
  "ds_read_b64_tr_b16 v[82:83], v60 offset:144\n" \
  "s_waitcnt lgkmcnt(5)\n" \
  "v_mfma_f32_16x16x32_bf16 v[12:15], v[92:95], v[48:51], v[12:15]\n" \
  "ds_read_b64_tr_b16 v[84:85], v60 offset:40\n" \
  "ds_read_b128 v[44:47], v62 offset:2048\n" \
  "s_lshr_b32 s72, s72, 3\n" \
  "v_mfma_f32_16x16x32_bf16 v[32:35], v[92:95], v[52:55], v[32:35]\n" \
  "ds_read_b64_tr_b16 v[86:87], v60 offset:184\n" \
  "s_waitcnt lgkmcnt(6)\n" \
  "v_mfma_f32_16x16x32_bf16 v[16:19], v[56:59], v[48:51], v[16:19]\n" \
  "ds_read_b64_tr_b16 v[88:89], v60 offset:72\n" \
  "s_sub_u32 s73, s73, 1\n" \
  "v_mfma_f32_16x16x32_bf16 v[36:39], v[56:59], v[52:55], v[36:39]\n" \
  "ds_read_b64_tr_b16 v[90:91], v60 offset:216\n" \
  "s_cmp_lg_u32 s73, 0\n" \
  "s_cbranch_scc1 Lg5l_%=\n" \
  "s_cmp_eq_u32 s5, 3\n" \
  "s_cbranch_scc1 Lepq_%=\n" \
  "s_branch Lepi_%=\n" \
    \
  "Lg5n_%=:\n" \
  "s_cmp_lg_u32 s73, 2\n" \
  "s_cbranch_scc1 Lg5b2_%=\n" \
  "s_waitcnt lgkmcnt(0)\n" \
  "s_barrier\n" \
  "s_getpc_b64 s[70:71]\n" \
  "s_branch Lsetup_%=\n" \
  "s_mul_i32 s36, s4, 4608\n" \
  "s_add_u32 s36, s36, 136192\n" \
  "v_lshl_add_u32 v102, v124, 4, s36\n" \
  "v_and_b32 v104, 31, v124\n" \
  "v_lshl_add_u32 v104, v104, 4, s36\n" \
  "s_barrier\n" \
  "ds_read_b128 v[64:67], v123\n" \
  "ds_read_b128 v[68:71], v123 offset:32\n" \
  "ds_read_b128 v[72:75], v123 offset:64\n" \
  "ds_read_b128 v[76:79], v123 offset:96\n" \
  "s_barrier\n" \
  "s_branch Lzw5_%=\n" \
  "Lzw4_%=:\n" \
  "s_sleep 1\n" \
  "Lzw5_%=:\n" \
  "ds_read_b32 v113, v121 offset:24\n" \
  "s_waitcnt lgkmcnt(0)\n" \
  "v_readfirstlane_b32 s47, v113\n" \
  "s_bitcmp1_b32 s47, 2\n" \
  "s_cbranch_scc0 Lzw4_%=\n" \
  "s_setprio 1\n" \
  "s_branch Lg5b2_%=\n" \
  "Lv0_%=:\n" \
  "s_waitcnt lgkmcnt(0)\n" \
  "s_barrier\n" \
  "ds_read_b64_tr_b16 v[80:81], v60\n" \
  "ds_read_b128 v[40:43], v62\n" \
  "ds_read_b64_tr_b16 v[82:83], v60 offset:144\n" \
  "ds_read_b64_tr_b16 v[84:85], v60 offset:40\n" \
  "ds_read_b128 v[44:47], v62 offset:2048\n" \
  "ds_read_b64_tr_b16 v[86:87], v60 offset:184\n" \
  "ds_read_b64_tr_b16 v[88:89], v60 offset:72\n" \
  "ds_read_b64_tr_b16 v[90:91], v60 offset:216\n" \
    \
  "Lg4l_%=:\n" \
  "s_waitcnt lgkmcnt(5)\n" \
  "v_mfma_f32_16x16x32_bf16 v[0:3], v[40:43], v[80:83], v[0:3]\n" \
  "ds_read_b64_tr_b16 v[92:93], v60 offset:112\n" \
  "ds_read_b128 v[48:51], v63\n" \
  "s_waitcnt lgkmcnt(5)\n" \
  "v_mfma_f32_16x16x32_bf16 v[20:23], v[44:47], v[80:83], v[20:23]\n" \
  "ds_read_b64_tr_b16 v[94:95], v60 offset:256\n" \
  "s_waitcnt lgkmcnt(5)\n" \
  "v_mfma_f32_16x16x32_bf16 v[4:7], v[40:43], v[84:87], v[4:7]\n" \
  "ds_read_b64_tr_b16 v[80:81], v60 offset:4608\n" \
  "ds_read_b128 v[52:55], v63 offset:2048\n" \
  "v_mfma_f32_16x16x32_bf16 v[24:27], v[44:47], v[84:87], v[24:27]\n" \
  "ds_read_b64_tr_b16 v[82:83], v60 offset:4752\n" \
  "s_waitcnt lgkmcnt(6)\n" \
  "v_mfma_f32_16x16x32_bf16 v[8:11], v[40:43], v[88:91], v[8:11]\n" \
  "ds_read_b64_tr_b16 v[84:85], v60 offset:4648\n" \
  "v_mfma_f32_16x16x32_bf16 v[28:31], v[44:47], v[88:91], v[28:31]\n" \
  "ds_read_b64_tr_b16 v[86:87], v60 offset:4792\n" \
  "s_waitcnt lgkmcnt(5)\n" \
  "v_mfma_f32_16x16x32_bf16 v[12:15], v[40:43], v[92:95], v[12:15]\n" \
  "ds_read_b64_tr_b16 v[88:89], v60 offset:4680\n" \
  "v_mfma_f32_16x16x32_bf16 v[32:35], v[44:47], v[92:95], v[32:35]\n" \
  "ds_read_b64_tr_b16 v[90:91], v60 offset:4824\n" \
  "s_waitcnt lgkmcnt(4)\n" \
  "v_mfma_f32_16x16x32_bf16 v[0:3], v[48:51], v[80:83], v[0:3]\n" \
  "ds_read_b64_tr_b16 v[92:93], v60 offset:4720\n" \
  "s_bitcmp1_b32 s72, 0\n" \
  "v_mfma_f32_16x16x32_bf16 v[20:23], v[52:55], v[80:83], v[20:23]\n" \
  "ds_read_b64_tr_b16 v[94:95], v60 offset:4864\n" \
  "s_cbranch_scc0 Lg4b0_%=\n" \
  "s_waitcnt lgkmcnt(0)\n" \
  "s_barrier\n" \
  "Lg4b0_%=:\n" \
  "s_waitcnt lgkmcnt(4)\n" \
  "v_mfma_f32_16x16x32_bf16 v[4:7], v[48:51], v[84:87], v[4:7]\n" \
  "ds_read_b64_tr_b16 v[80:81], v60 offset:27648\n" \
  "ds_read_b128 v[40:43], v62 offset:16384\n" \
  "v_mfma_f32_16x16x32_bf16 v[24:27], v[52:55], v[84:87], v[24:27]\n" \
  "ds_read_b64_tr_b16 v[82:83], v60 offset:27792\n" \
  "s_waitcnt lgkmcnt(5)\n" \
  "v_mfma_f32_16x16x32_bf16 v[8:11], v[48:51], v[88:91], v[8:11]\n" \
  "ds_read_b64_tr_b16 v[84:85], v60 offset:27688\n" \
  "ds_read_b128 v[44:47], v62 offset:18432\n" \
  "v_mfma_f32_16x16x32_bf16 v[28:31], v[52:55], v[88:91], v[28:31]\n" \
  "ds_read_b64_tr_b16 v[86:87], v60 offset:27832\n" \
  "s_waitcnt lgkmcnt(6)\n" \
  "v_mfma_f32_16x16x32_bf16 v[12:15], v[48:51], v[92:95], v[12:15]\n" \
  "ds_read_b64_tr_b16 v[88:89], v60 offset:27720\n" \
  "v_mfma_f32_16x16x32_bf16 v[32:35], v[52:55], v[92:95], v[32:35]\n" \
  "ds_read_b64_tr_b16 v[90:91], v60 offset:27864\n" \
  "s_waitcnt lgkmcnt(5)\n" \
  "v_mfma_f32_16x16x32_bf16 v[0:3], v[40:43], v[80:83], v[0:3]\n" \
  "ds_read_b64_tr_b16 v[92:93], v60 offset:27760\n" \
  "ds_read_b128 v[48:51], v63 offset:16384\n" \
  "s_waitcnt lgkmcnt(5)\n" \
  "v_mfma_f32_16x16x32_bf16 v[20:23], v[44:47], v[80:83], v[20:23]\n" \
  "ds_read_b64_tr_b16 v[94:95], v60 offset:27904\n" \
  "s_waitcnt lgkmcnt(5)\n" \
  "v_mfma_f32_16x16x32_bf16 v[4:7], v[40:43], v[84:87], v[4:7]\n" \
  "ds_read_b64_tr_b16 v[80:81], v60 offset:32256\n" \
  "ds_read_b128 v[52:55], v63 offset:18432\n" \
  "v_mfma_f32_16x16x32_bf16 v[24:27], v[44:47], v[84:87], v[24:27]\n" \
  "ds_read_b64_tr_b16 v[82:83], v60 offset:32400\n" \
  "s_waitcnt lgkmcnt(6)\n" \
  "v_mfma_f32_16x16x32_bf16 v[8:11], v[40:43], v[88:91], v[8:11]\n" \
  "ds_read_b64_tr_b16 v[84:85], v60 offset:32296\n" \
  "v_mfma_f32_16x16x32_bf16 v[28:31], v[44:47], v[88:91], v[28:31]\n" \
  "ds_read_b64_tr_b16 v[86:87], v60 offset:32440\n" \
  "s_waitcnt lgkmcnt(5)\n" \
  "v_mfma_f32_16x16x32_bf16 v[12:15], v[40:43], v[92:95], v[12:15]\n" \
  "ds_read_b64_tr_b16 v[88:89], v60 offset:32328\n" \
  "v_mfma_f32_16x16x32_bf16 v[32:35], v[44:47], v[92:95], v[32:35]\n" \
  "ds_read_b64_tr_b16 v[90:91], v60 offset:32472\n" \
  "s_waitcnt lgkmcnt(4)\n" \
  "v_mfma_f32_16x16x32_bf16 v[0:3], v[48:51], v[80:83], v[0:3]\n" \
  "ds_read_b64_tr_b16 v[92:93], v60 offset:32368\n" \
  "s_bitcmp1_b32 s72, 1\n" \
  "v_mfma_f32_16x16x32_bf16 v[20:23], v[52:55], v[80:83], v[20:23]\n" \
  "ds_read_b64_tr_b16 v[94:95], v60 offset:32512\n" \
  "s_cbranch_scc0 Lg4b1_%=\n" \
  "s_waitcnt lgkmcnt(0)\n" \
  "s_barrier\n" \
  "Lg4b1_%=:\n" \
  "s_waitcnt lgkmcnt(4)\n" \
  "v_mfma_f32_16x16x32_bf16 v[4:7], v[48:51], v[84:87], v[4:7]\n" \
  "ds_read_b64_tr_b16 v[80:81], v60 offset:55296\n" \
  "ds_read_b128 v[40:43], v98\n" \
  "v_mfma_f32_16x16x32_bf16 v[24:27], v[52:55], v[84:87], v[24:27]\n" \
  "ds_read_b64_tr_b16 v[82:83], v60 offset:55440\n" \
  "s_waitcnt lgkmcnt(5)\n" \
  "v_mfma_f32_16x16x32_bf16 v[8:11], v[48:51], v[88:91], v[8:11]\n" \
  "ds_read_b64_tr_b16 v[84:85], v60 offset:55336\n" \
  "ds_read_b128 v[44:47], v98 offset:2048\n" \
  "v_mfma_f32_16x16x32_bf16 v[28:31], v[52:55], v[88:91], v[28:31]\n" \
  "ds_read_b64_tr_b16 v[86:87], v60 offset:55480\n" \
  "s_waitcnt lgkmcnt(6)\n" \
  "v_mfma_f32_16x16x32_bf16 v[12:15], v[48:51], v[92:95], v[12:15]\n" \
  "ds_read_b64_tr_b16 v[88:89], v60 offset:55368\n" \
  "v_mfma_f32_16x16x32_bf16 v[32:35], v[52:55], v[92:95], v[32:35]\n" \
  "ds_read_b64_tr_b16 v[90:91], v60 offset:55512\n" \
  "s_waitcnt lgkmcnt(5)\n" \
  "v_mfma_f32_16x16x32_bf16 v[0:3], v[40:43], v[80:83], v[0:3]\n" \
  "ds_read_b64_tr_b16 v[92:93], v60 offset:55408\n" \
  "ds_read_b128 v[48:51], v99\n" \
  "s_waitcnt lgkmcnt(5)\n" \
  "v_mfma_f32_16x16x32_bf16 v[20:23], v[44:47], v[80:83], v[20:23]\n" \
  "ds_read_b64_tr_b16 v[94:95], v60 offset:55552\n" \
  "s_waitcnt lgkmcnt(5)\n" \
  "v_mfma_f32_16x16x32_bf16 v[4:7], v[40:43], v[84:87], v[4:7]\n" \
  "ds_read_b64_tr_b16 v[80:81], v60 offset:59904\n" \
  "ds_read_b128 v[52:55], v99 offset:2048\n" \
  "v_mfma_f32_16x16x32_bf16 v[24:27], v[44:47], v[84:87], v[24:27]\n" \
  "ds_read_b64_tr_b16 v[82:83], v60 offset:60048\n" \
  "s_waitcnt lgkmcnt(6)\n" \
  "v_mfma_f32_16x16x32_bf16 v[8:11], v[40:43], v[88:91], v[8:11]\n" \
  "ds_read_b64_tr_b16 v[84:85], v60 offset:59944\n" \
  "v_mfma_f32_16x16x32_bf16 v[28:31], v[44:47], v[88:91], v[28:31]\n" \
  "ds_read_b64_tr_b16 v[86:87], v60 offset:60088\n" \
  "s_waitcnt lgkmcnt(5)\n" \
  "v_mfma_f32_16x16x32_bf16 v[12:15], v[40:43], v[92:95], v[12:15]\n" \
  "ds_read_b64_tr_b16 v[88:89], v60 offset:59976\n" \
  "v_mfma_f32_16x16x32_bf16 v[32:35], v[44:47], v[92:95], v[32:35]\n" \
  "ds_read_b64_tr_b16 v[90:91], v60 offset:60120\n" \
  "s_waitcnt lgkmcnt(4)\n" \
  "v_mfma_f32_16x16x32_bf16 v[0:3], v[48:51], v[80:83], v[0:3]\n" \
  "ds_read_b64_tr_b16 v[92:93], v60 offset:60016\n" \
  "s_bitcmp1_b32 s72, 2\n" \
  "v_mfma_f32_16x16x32_bf16 v[20:23], v[52:55], v[80:83], v[20:23]\n" \
  "ds_read_b64_tr_b16 v[94:95], v60 offset:60160\n" \
  "s_cbranch_scc0 Lg4b2_%=\n" \
  "s_waitcnt lgkmcnt(0)\n" \
  "s_barrier\n" \
  "Lg4b2_%=:\n" \
  "s_waitcnt lgkmcnt(4)\n" \
  "v_mfma_f32_16x16x32_bf16 v[4:7], v[48:51], v[84:87], v[4:7]\n" \
  "ds_read_b64_tr_b16 v[80:81], v60\n" \
  "ds_read_b128 v[40:43], v62\n" \
  "v_mfma_f32_16x16x32_bf16 v[24:27], v[52:55], v[84:87], v[24:27]\n" \
  "ds_read_b64_tr_b16 v[82:83], v60 offset:144\n" \
  "s_waitcnt lgkmcnt(5)\n" \
  "v_mfma_f32_16x16x32_bf16 v[8:11], v[48:51], v[88:91], v[8:11]\n" \
  "ds_read_b64_tr_b16 v[84:85], v60 offset:40\n" \
  "ds_read_b128 v[44:47], v62 offset:2048\n" \
  "s_lshr_b32 s72, s72, 3\n" \
  "v_mfma_f32_16x16x32_bf16 v[28:31], v[52:55], v[88:91], v[28:31]\n" \
  "ds_read_b64_tr_b16 v[86:87], v60 offset:184\n" \
  "s_waitcnt lgkmcnt(6)\n" \
  "v_mfma_f32_16x16x32_bf16 v[12:15], v[48:51], v[92:95], v[12:15]\n" \
  "ds_read_b64_tr_b16 v[88:89], v60 offset:72\n" \
  "s_sub_u32 s73, s73, 1\n" \
  "v_mfma_f32_16x16x32_bf16 v[32:35], v[52:55], v[92:95], v[32:35]\n" \
  "ds_read_b64_tr_b16 v[90:91], v60 offset:216\n" \
  "s_cmp_lg_u32 s73, 0\n" \
  "s_cbranch_scc1 Lg4l_%=\n" \
  "s_setprio 3\n" \
  "s_nop 3\n" \
  "v_cvt_pk_bf16_f32 v0, v0, v1\n" \
  "v_cvt_pk_bf16_f32 v1, v2, v3\n" \
  "v_cvt_pk_bf16_f32 v2, v20, v21\n" \
  "v_cvt_pk_bf16_f32 v3, v22, v23\n" \
  "v_cvt_pk_bf16_f32 v4, v4, v5\n" \
  "v_cvt_pk_bf16_f32 v5, v6, v7\n" \
  "v_cvt_pk_bf16_f32 v6, v24, v25\n" \
  "v_cvt_pk_bf16_f32 v7, v26, v27\n" \
  "v_permlane32_swap_b32 v0, v2\n" \
  "v_permlane32_swap_b32 v1, v3\n" \
  "global_store_dwordx4 v103, v[0:3], s[26:27] sc1\n" \
  "v_cvt_pk_bf16_f32 v8, v8, v9\n" \
  "v_cvt_pk_bf16_f32 v9, v10, v11\n" \
  "v_cvt_pk_bf16_f32 v10, v28, v29\n" \
  "v_cvt_pk_bf16_f32 v11, v30, v31\n" \
  "v_permlane32_swap_b32 v4, v6\n" \
  "v_permlane32_swap_b32 v5, v7\n" \
  "global_store_dwordx4 v103, v[4:7], s[26:27] offset:1024 sc1\n" \
  "v_cvt_pk_bf16_f32 v12, v12, v13\n" \
  "v_cvt_pk_bf16_f32 v13, v14, v15\n" \
  "v_cvt_pk_bf16_f32 v14, v32, v33\n" \
  "v_cvt_pk_bf16_f32 v15, v34, v35\n" \
  "v_permlane32_swap_b32 v8, v10\n" \
  "v_permlane32_swap_b32 v9, v11\n" \
  "global_store_dwordx4 v103, v[8:11], s[26:27] offset:2048 sc1\n" \
  "s_nop 1\n" \
  "v_permlane32_swap_b32 v12, v14\n" \
  "v_permlane32_swap_b32 v13, v15\n" \
  "global_store_dwordx4 v103, v[12:15], s[26:27] offset:3072 sc1\n" \
  "s_branch Lep2_%=\n" \
  "Lt0_%=:\n" \
  "s_sleep 2\n" \
  "s_swappc_b64 s[70:71], s[70:71]\n" \
  "s_branch Lt1_%=\n" \
    \
  "Lepi_%=:\n" \
  "s_setprio 3\n" \
  "s_nop 7\n" \
  "v_cvt_pk_bf16_f32 v16, v16, v17\n" \
  "v_cvt_pk_bf16_f32 v17, v18, v19\n" \
  "v_cvt_pk_bf16_f32 v18, v36, v37\n" \
  "v_cvt_pk_bf16_f32 v19, v38, v39\n" \
  "s_nop 1\n" \
  "v_permlane16_swap_b32 v16, v18\n" \
  "v_permlane16_swap_b32 v17, v19\n" \
  "ds_write_b128 v105, v[16:19]\n" \
  "ds_read_b64_tr_b16 v[36:37], v100\n" \
  "ds_read_b64_tr_b16 v[38:39], v100 offset:128\n" \
  "v_cvt_pk_bf16_f32 v0, v0, v1\n" \
  "v_cvt_pk_bf16_f32 v1, v2, v3\n" \
  "v_cvt_pk_bf16_f32 v2, v20, v21\n" \
  "v_cvt_pk_bf16_f32 v3, v22, v23\n" \
  "v_cvt_pk_bf16_f32 v4, v4, v5\n" \
  "v_cvt_pk_bf16_f32 v5, v6, v7\n" \
  "v_cvt_pk_bf16_f32 v6, v24, v25\n" \
  "v_cvt_pk_bf16_f32 v7, v26, v27\n" \
  "v_permlane16_swap_b32 v0, v2\n" \
  "v_permlane16_swap_b32 v1, v3\n" \
  "global_store_dwordx4 v103, v[0:3], s[26:27] sc1\n" \
  "v_cvt_pk_bf16_f32 v8, v8, v9\n" \
  "v_cvt_pk_bf16_f32 v9, v10, v11\n" \
  "v_cvt_pk_bf16_f32 v10, v28, v29\n" \
  "v_cvt_pk_bf16_f32 v11, v30, v31\n" \
  "v_permlane16_swap_b32 v4, v6\n" \
  "v_permlane16_swap_b32 v5, v7\n" \
  "global_store_dwordx4 v103, v[4:7], s[26:27] offset:1024 sc1\n" \
  "v_cvt_pk_bf16_f32 v12, v12, v13\n" \
  "v_cvt_pk_bf16_f32 v13, v14, v15\n" \
  "v_cvt_pk_bf16_f32 v14, v32, v33\n" \
  "v_cvt_pk_bf16_f32 v15, v34, v35\n" \
  "v_permlane16_swap_b32 v8, v10\n" \
  "v_permlane16_swap_b32 v9, v11\n" \
  "global_store_dwordx4 v103, v[8:11], s[26:27] offset:2048 sc1\n" \
  "s_nop 1\n" \
  "v_permlane16_swap_b32 v12, v14\n" \
  "v_permlane16_swap_b32 v13, v15\n" \
  "global_store_dwordx4 v103, v[12:15], s[26:27] offset:3072 sc1\n" \
  "s_mov_b32 exec_hi, 0\n" \
  "global_store_dwordx4 v104, v[16:19], s[26:27] sc1\n" \
  "s_mov_b32 exec_lo, 0x00ff00ff\n" \
  "s_mov_b32 exec_hi, 0x00ff00ff\n" \
  "s_waitcnt lgkmcnt(0)\n" \
  "global_store_dwordx4 v101, v[36:39], s[26:27] sc1\n" \
  "ds_write_b128 v101, v[36:39] offset:46080\n" \
  "s_mov_b64 exec, -1\n" \
  "Lep2_%=:\n" \
  "s_setprio 0\n" \
  "ds_write_b128 v102, v[0:3]\n" \
  "ds_write_b128 v102, v[4:7] offset:1024\n" \
  "ds_write_b128 v102, v[8:11] offset:2048\n" \
  "ds_write_b128 v102, v[12:15] offset:3072\n" \
    \
  "Lhelp_%=:\n" \
  "s_setprio 3\n" \
  "ds_read_b64 v[68:69], v65\n" \
  "s_mov_b64 exec, 1\n" \
  "ds_add_u32 v121, v64 offset:76\n" \
  "s_mov_b64 exec, -1\n" \
  "s_waitcnt lgkmcnt(0)\n" \
  "v_readlane_b32 s14, v68, s7\n" \
  "v_readlane_b32 s15, v69, s7\n" \
  "s_lshl_b32 s44, 6, s7\n" \
  "s_and_b32 s44, s44, 0xffff\n" \
  "s_mov_b32 s45, 0\n" \
  "s_waitcnt vmcnt(0)\n" \
  "s_mov_b64 exec, 1\n" \
  "ds_add_rtn_u32 v65, v121, v64 offset:32\n" \
  "s_mov_b64 exec, -1\n" \
  "s_waitcnt lgkmcnt(0)\n" \
  "v_readfirstlane_b32 s42, v65\n" \
  "s_cmp_lg_u32 s42, 7\n" \
  "s_cbranch_scc1 Lh2_%=\n" \
  "s_cmp_eq_u32 s7, 15\n" \
  "s_cbranch_scc1 Lh3_%=\n" \
  "s_mov_b64 exec, s[44:45]\n" \
  "global_atomic_swap_x2 v66, v[68:69], s[24:25] offset:120 sc1\n" \
  "s_mov_b64 exec, -1\n" \
  "Lh3_%=:\n" \
    \
  "Lh2_%=:\n" \
  "s_cmp_eq_u32 s10, 0\n" \
  "s_cbranch_scc1 Lwo_%=\n" \
  "s_cmp_eq_u32 s7, 0\n" \
  "s_cbranch_scc1 Lpf2_%=\n" \
  "s_cmp_gt_u32 s7, 1\n" \
  "s_cselect_b32 s43, 3, 1\n" \
  "s_mov_b32 s47, 0\n" \
  "s_mov_b32 s46, s43\n" \
  "s_and_b32 s36, s4, 1\n" \
  "s_lshl_b32 s36, s36, 12\n" \
  "s_cmp_eq_u32 s4, 3\n" \
  "s_cmov_b32 s36, 4608\n" \
  "s_cmp_lt_u32 s4, 2\n" \
  "s_cselect_b32 s48, -1, 0\n" \
  "s_mov_b32 s37, 18432\n" \
  "s_cmov_b32 s37, 9216\n" \
  "s_mov_b32 s38, 82944\n" \
  "s_cmov_b32 s38, 27648\n" \
  "s_mov_b32 s39, 9216\n" \
  "s_cmov_b32 s39, -9216\n" \
  "s_add_u32 s37, s37, s36\n" \
  "s_add_u32 s38, s38, s36\n" \
  "s_sub_u32 s37, 295936, s37\n" \
  "s_sub_u32 s26, s26, s37\n" \
  "s_subb_u32 s27, s27, 0\n" \
  "s_mov_b32 s40, s39\n" \
  "s_sub_u32 s41, -295936, s39\n" \
  "s_add_u32 s44, s4, 1\n" \
  "s_bitcmp1_b32 s44, 1\n" \
  "s_cbranch_scc0 Lzg6_%=\n" \
  "v_mov_b32 v64, 0x11\n" \
  "s_mov_b32 s44, 0x100000\n" \
  "s_mov_b64 exec, s[46:47]\n" \
  "global_atomic_add_x2 v[100:101], v107, v[76:77], s[24:25] offset:128 sc0 sc1\n" \
  "s_nop 15\n" \
  "s_nop 15\n" \
  "s_nop 15\n" \
  "s_nop 15\n" \
  "Lzp8_%=:\n" \
  "global_atomic_add_x2 v[102:103], v107, v[76:77], s[24:25] offset:128 sc0 sc1\n" \
  "s_sub_u32 s44, s44, 1\n" \
  "s_cbranch_scc1 Lzp9_%=\n" \
  "s_waitcnt vmcnt(1)\n" \
  "v_cmp_ne_u64 vcc, s[14:15], v[100:101]\n" \
  "s_cmp_eq_u64 vcc, 0\n" \
  "s_cbranch_scc1 Lzp9_%=\n" \
  "global_atomic_add_x2 v[100:101], v107, v[76:77], s[24:25] offset:128 sc0 sc1\n" \
  "s_waitcnt vmcnt(1)\n" \
  "v_cmp_ne_u64 vcc, s[14:15], v[102:103]\n" \
  "s_cmp_eq_u64 vcc, 0\n" \
  "s_cbranch_scc0 Lzp8_%=\n" \
  "Lzp9_%=:\n" \
  "s_mov_b64 exec, -1\n" \
  "ds_write_b32 v121, v125 offset:72\n" \
  "s_branch Lzg7_%=\n" \
  "Lzg6_%=:\n" \
  "s_branch Lzw11_%=\n" \
  "Lzw10_%=:\n" \
  "s_sleep 1\n" \
  "Lzw11_%=:\n" \
  "ds_read_b32 v0, v121 offset:72\n" \
  "s_waitcnt lgkmcnt(0)\n" \
  "v_readfirstlane_b32 s44, v0\n" \
  "s_bitcmp1_b32 s44, 0\n" \
  "s_cbranch_scc0 Lzw10_%=\n" \
  "Lzg7_%=:\n" \
  "s_branch Lzw13_%=\n" \
  "Lzw12_%=:\n" \
  "s_sleep 1\n" \
  "Lzw13_%=:\n" \
  "ds_read_b32 v0, v121 offset:76\n" \
  "s_waitcnt lgkmcnt(0)\n" \
  "v_readfirstlane_b32 s44, v0\n" \
  "s_bitcmp1_b32 s44, 4\n" \
  "s_cbranch_scc0 Lzw12_%=\n" \
  "Lhs_%=:\n" \
  "s_mov_b32 m0, s38\n" \
  "s_ashr_i32 s44, s40, 31\n" \
  "global_load_lds_dwordx4 v112, s[26:27] sc1\n" \
  "global_load_lds_dwordx4 v112, s[26:27] offset:1024 sc1\n" \
  "global_load_lds_dwordx4 v112, s[26:27] offset:2048 sc1\n" \
  "global_load_lds_dwordx4 v112, s[26:27] offset:3072 sc1\n" \
  "s_add_u32 m0, s38, 4096\n" \
  "s_add_u32 s38, s38, s39\n" \
  "s_cmp_eq_u32 s4, 0\n" \
  "s_cbranch_scc1 Lzh14_%=\n" \
  "s_mov_b32 exec_hi, s48\n" \
  "global_load_lds_dwordx4 v78, s[26:27] sc1\n" \
  "s_mov_b32 exec_hi, -1\n" \
  "Lzh14_%=:\n" \
  "s_add_u32 s26, s26, s40\n" \
  "s_addc_u32 s27, s27, s44\n" \
  "s_mov_b32 s44, s40\n" \
  "s_mov_b32 s40, s41\n" \
  "s_mov_b32 s41, s44\n" \
  "s_waitcnt vmcnt(0)\n" \
  "s_mov_b64 exec, 1\n" \
  "ds_add_u32 v121, v64 offset:36\n" \
  "s_mov_b64 exec, -1\n" \
  "v_lshlrev_b32 v64, 8, v64\n" \
  "s_sub_u32 s43, s43, 1\n" \
  "s_cbranch_scc0 Lhs_%=\n" \
  "Lpf2_%=:\n" \
  "s_branch Lop_%=\n" \
  "Lt1_%=:\n" \
  "s_sleep 2\n" \
  "s_swappc_b64 s[70:71], s[70:71]\n" \
  "s_branch Lt2_%=\n" \
    \
  "Lwo_%=:\n" \
  "s_branch Lzw16_%=\n" \
  "Lzw15_%=:\n" \
  "s_sleep 4\n" \
  "Lzw16_%=:\n" \
  "ds_read_b32 v40, v121 offset:32\n" \
  "s_waitcnt lgkmcnt(0)\n" \
  "v_readfirstlane_b32 s44, v40\n" \
  "s_bitcmp1_b32 s44, 3\n" \
  "s_cbranch_scc0 Lzw15_%=\n" \
  "s_lshl_b32 s36, s61, 2\n" \
  "s_xor_b32 s36, s36, s7\n" \
  "s_mul_i32 s36, s36, 4608\n" \
  "s_lshl_b32 s37, s4, 10\n" \
  "s_add_u32 s36, s36, s37\n" \
  "s_mov_b32 s40, 0x00ff00ff\n" \
  "s_mov_b32 s41, 0x00ff00ff\n" \
  "v_subrev_u32 v41, 36864, v122\n" \
  "s_add_u32 s38, s62, s36\n" \
  "s_addc_u32 s39, s63, 0\n" \
  "s_branch Lzw18_%=\n" \
  "Lzw17_%=:\n" \
  "s_sleep 2\n" \
  "Lzw18_%=:\n" \
  "ds_read_b32 v40, v121 offset:60\n" \
  "s_waitcnt lgkmcnt(0)\n" \
  "v_readfirstlane_b32 s42, v40\n" \
  "s_bitcmp1_b32 s42, 6\n" \
  "s_cbranch_scc0 Lzw17_%=\n" \
  "ds_read_b64_tr_b16 v[44:45], v70\n" \
  "ds_read_b64_tr_b16 v[46:47], v70 offset:576\n" \
  "ds_read_b64_tr_b16 v[48:49], v70 offset:128\n" \
  "ds_read_b64_tr_b16 v[50:51], v70 offset:704\n" \
  "ds_read_b64_tr_b16 v[52:53], v71\n" \
  "ds_read_b64_tr_b16 v[54:55], v71 offset:128\n" \
  "s_waitcnt lgkmcnt(0)\n" \
  "global_store_dwordx4 v112, v[44:47], s[38:39] sc1\n" \
  "s_cmp_lg_u32 s4, 0\n" \
  "s_cbranch_scc1 Lwp1_%=\n" \
  "s_mov_b64 exec, s[40:41]\n" \
  "global_store_dwordx4 v41, v[48:51], s[38:39] sc1\n" \
  "Lwp1_%=:\n" \
  "s_cmp_lg_u32 s4, 1\n" \
  "s_cbranch_scc1 Lwp2_%=\n" \
  "s_cmp_ge_u32 s7, 10\n" \
  "s_cbranch_scc1 Lwp2_%=\n" \
  "s_cmp_ge_u32 s7, 5\n" \
  "s_mov_b32 s36, 73728\n" \
  "s_cmov_b32 s36, 78336\n" \
  "s_cselect_b32 s37, 5, 0\n" \
  "s_sub_u32 s37, s7, s37\n" \
  "s_add_u32 s38, s62, s36\n" \
  "s_addc_u32 s39, s63, 0\n" \
  "s_cmp_eq_u32 s37, 4\n" \
  "s_cselect_b64 exec, s[40:41], -1\n" \
  "s_cbranch_scc1 Lwp3_%=\n" \
  "s_lshl_b32 s37, s37, 10\n" \
  "v_add_u32 v41, s37, v112\n" \
  "Lwp3_%=:\n" \
  "global_store_dwordx4 v41, v[52:55], s[38:39] sc1\n" \
  "Lwp2_%=:\n" \
  "s_mov_b64 exec, -1\n" \
  "s_waitcnt vmcnt(0)\n" \
  "s_mov_b64 exec, 1\n" \
  "ds_add_rtn_u32 v65, v121, v64 offset:28\n" \
  "s_mov_b64 exec, -1\n" \
  "s_waitcnt lgkmcnt(0)\n" \
  "v_readfirstlane_b32 s42, v65\n" \
  "s_cmp_lg_u32 s42, 3\n" \
  "s_cbranch_scc1 Lwp4_%=\n" \
  "s_mov_b64 exec, 0xffff\n" \
  "global_atomic_swap_x2 v67, v[68:69], s[24:25] offset:512 sc1\n" \
  "s_mov_b64 exec, -1\n" \
  "Lwp4_%=:\n" \
  "s_cmp_eq_u32 s7, 0\n" \
  "s_cselect_b32 s43, 7, 2\n" \
  "s_cselect_b32 s45, 7, 2\n" \
  "s_cmp_gt_u32 s7, 1\n" \
  "s_cselect_b32 s43, 18, s43\n" \
  "s_branch Lzw20_%=\n" \
  "Lzw19_%=:\n" \
  "s_sleep 4\n" \
  "Lzw20_%=:\n" \
  "ds_read_b32 v65, v121 offset:36\n" \
  "s_waitcnt lgkmcnt(0)\n" \
  "v_readfirstlane_b32 s42, v65\n" \
  "s_bitcmp1_b32 s42, s45\n" \
  "s_cbranch_scc0 Lzw19_%=\n" \
  "s_cmp_lg_u32 s4, 0\n" \
  "s_cbranch_scc1 Lzg21_%=\n" \
  "s_mov_b32 s44, 0x100000\n" \
  "s_mov_b64 exec, 0xffff\n" \
  "global_atomic_add_x2 v[100:101], v107, v[76:77], s[24:25] offset:512 sc0 sc1\n" \
  "s_nop 15\n" \
  "s_nop 15\n" \
  "s_nop 15\n" \
  "s_nop 15\n" \
  "Lzp23_%=:\n" \
  "global_atomic_add_x2 v[102:103], v107, v[76:77], s[24:25] offset:512 sc0 sc1\n" \
  "s_sub_u32 s44, s44, 1\n" \
  "s_cbranch_scc1 Lzp24_%=\n" \
  "s_waitcnt vmcnt(1)\n" \
  "v_cmp_ne_u64 vcc, s[14:15], v[100:101]\n" \
  "s_cmp_eq_u64 vcc, 0\n" \
  "s_cbranch_scc1 Lzp24_%=\n" \
  "global_atomic_add_x2 v[100:101], v107, v[76:77], s[24:25] offset:512 sc0 sc1\n" \
  "s_waitcnt vmcnt(1)\n" \
  "v_cmp_ne_u64 vcc, s[14:15], v[102:103]\n" \
  "s_cmp_eq_u64 vcc, 0\n" \
  "s_cbranch_scc0 Lzp23_%=\n" \
  "Lzp24_%=:\n" \
  "s_mov_b64 exec, -1\n" \
  "ds_write_b32 v121, v125 offset:24\n" \
  "s_branch Lzg22_%=\n" \
  "Lzg21_%=:\n" \
  "s_branch Lzw26_%=\n" \
  "Lzw25_%=:\n" \
  "s_sleep 4\n" \
  "Lzw26_%=:\n" \
  "ds_read_b32 v0, v121 offset:24\n" \
  "s_waitcnt lgkmcnt(0)\n" \
  "v_readfirstlane_b32 s44, v0\n" \
  "s_bitcmp1_b32 s44, 0\n" \
  "s_cbranch_scc0 Lzw25_%=\n" \
  "Lzg22_%=:\n" \
  "s_branch Lzw28_%=\n" \
  "Lzw27_%=:\n" \
  "s_sleep 2\n" \
  "Lzw28_%=:\n" \
  "ds_read_b32 v65, v121 offset:36\n" \
  "s_waitcnt lgkmcnt(0)\n" \
  "v_readfirstlane_b32 s42, v65\n" \
  "s_bitcmp1_b32 s42, s43\n" \
  "s_cbranch_scc0 Lzw27_%=\n" \
  "s_branch Lzw30_%=\n" \
  "Lzw29_%=:\n" \
  "s_sleep 2\n" \
  "Lzw30_%=:\n" \
  "ds_read_b32 v65, v121 offset:40\n" \
  "s_waitcnt lgkmcnt(0)\n" \
  "v_readfirstlane_b32 s42, v65\n" \
  "s_bitcmp1_b32 s42, 3\n" \
  "s_cbranch_scc0 Lzw29_%=\n" \
  "s_mul_i32 s37, s8, 5\n" \
  "s_mov_b32 s38, 20\n" \
  "Lzo31_%=:\n" \
  "s_lshl_b32 s39, s37, 2\n" \
  "s_add_u32 s39, s39, s4\n" \
  "s_min_u32 s39, s39, 80\n" \
  "s_cmp_lt_u32 s39, 45\n" \
  "s_mov_b32 s42, 73728\n" \
  "s_cmov_b32 s42, 36864\n" \
  "s_lshl_b32 s39, s39, 10\n" \
  "s_add_u32 m0, s39, s42\n" \
  "s_add_u32 s44, s62, s39\n" \
  "s_addc_u32 s45, s63, 0\n" \
  "global_load_lds_dwordx4 v112, s[44:45] sc1\n" \
  "s_add_u32 s37, s37, 1\n" \
  "s_cmp_eq_u32 s37, 21\n" \
  "s_cselect_b32 s37, 0, s37\n" \
  "s_sub_u32 s38, s38, 1\n" \
  "s_cbranch_scc0 Lzo31_%=\n" \
  "s_waitcnt vmcnt(0)\n" \
    \
  "Lwo2_%=:\n" \
  "s_mov_b64 exec, 1\n" \
  "ds_add_u32 v121, v64 offset:52\n" \
  "s_mov_b64 exec, -1\n" \
  "s_mul_i32 s36, s6, 4480\n" \
  "s_add_u32 s38, s22, s36\n" \
  "s_addc_u32 s39, s23, 0\n" \
  "s_load_dwordx2 s[92:93], s[38:39], 1084\n" \
  "s_load_dwordx2 s[92:93], s[38:39], 3388\n" \
  "s_sleep 26\n" \
  "s_branch Lzw33_%=\n" \
  "Lzw32_%=:\n" \
  "s_sleep 4\n" \
  "Lzw33_%=:\n" \
  "ds_read_b32 v0, v121 offset:52\n" \
  "s_waitcnt lgkmcnt(0)\n" \
  "v_readfirstlane_b32 s42, v0\n" \
  "s_bitcmp1_b32 s42, 2\n" \
  "s_cbranch_scc0 Lzw32_%=\n" \
  "ds_read_b128 v[20:23], v113\n" \
  "ds_read_b128 v[24:27], v113 offset:1024\n" \
  "ds_read_b128 v[28:31], v113 offset:2048\n" \
  "ds_read_b128 v[32:35], v113 offset:3072\n" \
  "s_branch Lzw35_%=\n" \
  "Lzw34_%=:\n" \
  "s_sleep 4\n" \
  "Lzw35_%=:\n" \
  "ds_read_b32 v0, v121 offset:56\n" \
  "s_waitcnt lgkmcnt(0)\n" \
  "v_readfirstlane_b32 s42, v0\n" \
  "s_bitcmp1_b32 s42, 3\n" \
  "s_cbranch_scc0 Lzw34_%=\n" \
  "s_cmp_lg_u32 s4, 0\n" \
  "s_cbranch_scc1 Lo4_%=\n" \
  "s_branch Lzw37_%=\n" \
  "Lzw36_%=:\n" \
  "s_sleep 0\n" \
  "Lzw37_%=:\n" \
  "ds_read_b32 v0, v121 offset:44\n" \
  "s_waitcnt lgkmcnt(0)\n" \
  "v_readfirstlane_b32 s42, v0\n" \
  "s_bitcmp1_b32 s42, 2\n" \
  "s_cbranch_scc0 Lzw36_%=\n" \
  "s_nop 15\n" \
  "s_nop 15\n" \
  "Lo4_%=:\n" \
  "s_cmp_lg_u32 s4, 0\n" \
  "s_cbranch_scc1 Lzg38_%=\n" \
  "s_mov_b32 s44, 0x100000\n" \
  "s_mov_b64 exec, 0xff\n" \
  "global_atomic_add_x2 v[100:101], v107, v[76:77], s[24:25] offset:256 sc0 sc1\n" \
  "s_nop 15\n" \
  "s_nop 15\n" \
  "s_nop 15\n" \
  "s_nop 15\n" \
  "Lzp40_%=:\n" \
  "global_atomic_add_x2 v[102:103], v107, v[76:77], s[24:25] offset:256 sc0 sc1\n" \
  "s_sub_u32 s44, s44, 1\n" \
  "s_cbranch_scc1 Lzp41_%=\n" \
  "s_waitcnt vmcnt(1)\n" \
  "v_cmp_ne_u64 vcc, s[14:15], v[100:101]\n" \
  "s_cmp_eq_u64 vcc, 0\n" \
  "s_cbranch_scc1 Lzp41_%=\n" \
  "global_atomic_add_x2 v[100:101], v107, v[76:77], s[24:25] offset:256 sc0 sc1\n" \
  "s_waitcnt vmcnt(1)\n" \
  "v_cmp_ne_u64 vcc, s[14:15], v[102:103]\n" \
  "s_cmp_eq_u64 vcc, 0\n" \
  "s_cbranch_scc0 Lzp40_%=\n" \
  "Lzp41_%=:\n" \
  "s_mov_b64 exec, -1\n" \
  "ds_write_b32 v121, v125 offset:64\n" \
  "s_branch Lzg39_%=\n" \
  "Lzg38_%=:\n" \
  "s_branch Lzw43_%=\n" \
  "Lzw42_%=:\n" \
  "s_sleep 0\n" \
  "Lzw43_%=:\n" \
  "ds_read_b32 v0, v121 offset:64\n" \
  "s_waitcnt lgkmcnt(0)\n" \
  "v_readfirstlane_b32 s44, v0\n" \
  "s_bitcmp1_b32 s44, 0\n" \
  "s_cbranch_scc0 Lzw42_%=\n" \
  "Lzg39_%=:\n" \
  "Lo7_%=:\n" \
  "global_load_dwordx4 v[36:39], v112, s[64:65] sc1\n" \
  "s_nop 8\n" \
  "global_load_dwordx4 v[40:43], v112, s[64:65] offset:1024 sc1\n" \
  "s_nop 8\n" \
  "global_load_dwordx4 v[44:47], v112, s[64:65] offset:2048 sc1\n" \
  "s_nop 8\n" \
  "global_load_dwordx4 v[48:51], v112, s[64:65] offset:3072 sc1\n" \
  "s_nop 8\n" \
  "global_load_dwordx4 v[52:55], v112, s[66:67] sc1\n" \
  "s_nop 8\n" \
  "global_load_dwordx4 v[56:59], v112, s[66:67] offset:1024 sc1\n" \
  "s_nop 8\n" \
  "global_load_dwordx4 v[60:63], v112, s[66:67] offset:2048 sc1\n" \
  "s_nop 8\n" \
  "global_load_dwordx4 v[64:67], v112, s[66:67] offset:3072 sc1\n" \
  "s_branch Lo1_%=\n" \
  "Lt2_%=:\n" \
  "s_sleep 2\n" \
  "s_swappc_b64 s[70:71], s[70:71]\n" \
  "s_branch Lt3_%=\n" \
    \
  "Lop_%=:\n" \
  "s_mul_i32 s36, s6, 4480\n" \
  "s_add_u32 s38, s22, s36\n" \
  "s_addc_u32 s39, s23, 0\n" \
  "s_load_dwordx2 s[92:93], s[38:39], 1084\n" \
  "s_load_dwordx2 s[92:93], s[38:39], 3388\n" \
  "s_sleep 24\n" \
  "s_cmp_lg_u32 s4, 0\n" \
  "s_cbranch_scc1 Lo6_%=\n" \
  "s_branch Lzw45_%=\n" \
  "Lzw44_%=:\n" \
  "s_sleep 2\n" \
  "Lzw45_%=:\n" \
  "ds_read_b32 v0, v121 offset:48\n" \
  "s_waitcnt lgkmcnt(0)\n" \
  "v_readfirstlane_b32 s42, v0\n" \
  "s_bitcmp1_b32 s42, 2\n" \
  "s_cbranch_scc0 Lzw44_%=\n" \
  "Lo6_%=:\n" \
  "s_cmp_lg_u32 s4, 0\n" \
  "s_cbranch_scc1 Lzg46_%=\n" \
  "s_mov_b32 s44, 0x100000\n" \
  "s_mov_b64 exec, 0xff\n" \
  "global_atomic_add_x2 v[100:101], v107, v[76:77], s[24:25] offset:384 sc0 sc1\n" \
  "s_nop 15\n" \
  "s_nop 15\n" \
  "s_nop 15\n" \
  "s_nop 15\n" \
  "Lzp48_%=:\n" \
  "global_atomic_add_x2 v[102:103], v107, v[76:77], s[24:25] offset:384 sc0 sc1\n" \
  "s_sub_u32 s44, s44, 1\n" \
  "s_cbranch_scc1 Lzp49_%=\n" \
  "s_waitcnt vmcnt(1)\n" \
  "v_cmp_ne_u64 vcc, s[14:15], v[100:101]\n" \
  "s_cmp_eq_u64 vcc, 0\n" \
  "s_cbranch_scc1 Lzp49_%=\n" \
  "global_atomic_add_x2 v[100:101], v107, v[76:77], s[24:25] offset:384 sc0 sc1\n" \
  "s_waitcnt vmcnt(1)\n" \
  "v_cmp_ne_u64 vcc, s[14:15], v[102:103]\n" \
  "s_cmp_eq_u64 vcc, 0\n" \
  "s_cbranch_scc0 Lzp48_%=\n" \
  "Lzp49_%=:\n" \
  "s_mov_b64 exec, -1\n" \
  "ds_write_b32 v121, v125 offset:68\n" \
  "s_branch Lzg47_%=\n" \
  "Lzg46_%=:\n" \
  "s_branch Lzw51_%=\n" \
  "Lzw50_%=:\n" \
  "s_sleep 2\n" \
  "Lzw51_%=:\n" \
  "ds_read_b32 v0, v121 offset:68\n" \
  "s_waitcnt lgkmcnt(0)\n" \
  "v_readfirstlane_b32 s44, v0\n" \
  "s_bitcmp1_b32 s44, 0\n" \
  "s_cbranch_scc0 Lzw50_%=\n" \
  "Lzg47_%=:\n" \
  "global_load_dwordx4 v[36:39], v112, s[64:65] sc1\n" \
  "global_load_dwordx4 v[40:43], v112, s[64:65] offset:1024 sc1\n" \
  "global_load_dwordx4 v[44:47], v112, s[64:65] offset:2048 sc1\n" \
  "global_load_dwordx4 v[48:51], v112, s[64:65] offset:3072 sc1\n" \
  "global_load_dwordx4 v[52:55], v112, s[66:67] sc1\n" \
  "global_load_dwordx4 v[56:59], v112, s[66:67] offset:1024 sc1\n" \
  "global_load_dwordx4 v[60:63], v112, s[66:67] offset:2048 sc1\n" \
  "global_load_dwordx4 v[64:67], v112, s[66:67] offset:3072 sc1\n" \
  "s_branch Lzw53_%=\n" \
  "Lzw52_%=:\n" \
  "s_sleep 2\n" \
  "Lzw53_%=:\n" \
  "ds_read_b32 v0, v121 offset:52\n" \
  "s_waitcnt lgkmcnt(0)\n" \
  "v_readfirstlane_b32 s42, v0\n" \
  "s_bitcmp1_b32 s42, 2\n" \
  "s_cbranch_scc0 Lzw52_%=\n" \
  "ds_read_b128 v[20:23], v113\n" \
  "ds_read_b128 v[24:27], v113 offset:1024\n" \
  "ds_read_b128 v[28:31], v113 offset:2048\n" \
  "ds_read_b128 v[32:35], v113 offset:3072\n" \
  "s_branch Lzw55_%=\n" \
  "Lzw54_%=:\n" \
  "s_sleep 2\n" \
  "Lzw55_%=:\n" \
  "ds_read_b32 v0, v121 offset:56\n" \
  "s_waitcnt lgkmcnt(0)\n" \
  "v_readfirstlane_b32 s42, v0\n" \
  "s_bitcmp1_b32 s42, 3\n" \
  "s_cbranch_scc0 Lzw54_%=\n" \
  "s_setprio 0\n" \
    \
  "Lo1_%=:\n" \
  "s_waitcnt vmcnt(7) lgkmcnt(3)\n" \
  "v_mfma_f32_16x16x32_bf16 v[0:3], v[20:23], v[36:39], 0\n" \
  "global_load_dwordx4 v[68:71], v112, s[68:69] sc1\n" \
  "ds_read_b128 v[108:111], v122\n" \
  "s_waitcnt  lgkmcnt(3)\n" \
  "v_mfma_f32_16x16x32_bf16 v[4:7], v[24:27], v[36:39], 0\n" \
  "ds_read_b128 v[20:23], v113 offset:4608\n" \
  "s_waitcnt  lgkmcnt(3)\n" \
  "v_mfma_f32_16x16x32_bf16 v[8:11], v[28:31], v[36:39], 0\n" \
  "ds_read_b128 v[24:27], v113 offset:5632\n" \
  "s_waitcnt  lgkmcnt(3)\n" \
  "v_mfma_f32_16x16x32_bf16 v[12:15], v[32:35], v[36:39], 0\n" \
  "ds_read_b128 v[28:31], v113 offset:6656\n" \
  "s_waitcnt  lgkmcnt(3)\n" \
  "v_mfma_f32_16x16x32_bf16 v[16:19], v[108:111], v[36:39], 0\n" \
  "ds_read_b128 v[32:35], v113 offset:7680\n" \
  "s_waitcnt vmcnt(7) lgkmcnt(3)\n" \
  "v_mfma_f32_16x16x32_bf16 v[0:3], v[20:23], v[40:43], v[0:3]\n" \
  "global_load_dwordx4 v[72:75], v112, s[68:69] offset:1024 sc1\n" \
  "ds_read_b128 v[108:111], v122 offset:4608\n" \
  "s_waitcnt  lgkmcnt(3)\n" \
  "v_mfma_f32_16x16x32_bf16 v[4:7], v[24:27], v[40:43], v[4:7]\n" \
  "ds_read_b128 v[20:23], v113 offset:9216\n" \
  "s_waitcnt  lgkmcnt(3)\n" \
  "v_mfma_f32_16x16x32_bf16 v[8:11], v[28:31], v[40:43], v[8:11]\n" \
  "ds_read_b128 v[24:27], v113 offset:10240\n" \
  "s_waitcnt  lgkmcnt(3)\n" \
  "v_mfma_f32_16x16x32_bf16 v[12:15], v[32:35], v[40:43], v[12:15]\n" \
  "ds_read_b128 v[28:31], v113 offset:11264\n" \
  "s_waitcnt  lgkmcnt(3)\n" \
  "v_mfma_f32_16x16x32_bf16 v[16:19], v[108:111], v[40:43], v[16:19]\n" \
  "ds_read_b128 v[32:35], v113 offset:12288\n" \
  "s_waitcnt vmcnt(7) lgkmcnt(3)\n" \
  "v_mfma_f32_16x16x32_bf16 v[0:3], v[20:23], v[44:47], v[0:3]\n" \
  "global_load_dwordx4 v[76:79], v112, s[68:69] offset:2048 sc1\n" \
  "ds_read_b128 v[108:111], v122 offset:9216\n" \
  "s_waitcnt  lgkmcnt(3)\n" \
  "v_mfma_f32_16x16x32_bf16 v[4:7], v[24:27], v[44:47], v[4:7]\n" \
  "ds_read_b128 v[20:23], v113 offset:13824\n" \
  "s_waitcnt  lgkmcnt(3)\n" \
  "v_mfma_f32_16x16x32_bf16 v[8:11], v[28:31], v[44:47], v[8:11]\n" \
  "ds_read_b128 v[24:27], v113 offset:14848\n" \
  "s_waitcnt  lgkmcnt(3)\n" \
  "v_mfma_f32_16x16x32_bf16 v[12:15], v[32:35], v[44:47], v[12:15]\n" \
  "ds_read_b128 v[28:31], v113 offset:15872\n" \
  "s_waitcnt  lgkmcnt(3)\n" \
  "v_mfma_f32_16x16x32_bf16 v[16:19], v[108:111], v[44:47], v[16:19]\n" \
  "ds_read_b128 v[32:35], v113 offset:16896\n" \
  "s_waitcnt vmcnt(7) lgkmcnt(3)\n" \
  "v_mfma_f32_16x16x32_bf16 v[0:3], v[20:23], v[48:51], v[0:3]\n" \
  "global_load_dwordx4 v[80:83], v112, s[68:69] offset:3072 sc1\n" \
  "ds_read_b128 v[108:111], v122 offset:13824\n" \
  "s_waitcnt  lgkmcnt(3)\n" \
  "v_mfma_f32_16x16x32_bf16 v[4:7], v[24:27], v[48:51], v[4:7]\n" \
  "ds_read_b128 v[20:23], v113 offset:18432\n" \
  "s_waitcnt  lgkmcnt(3)\n" \
  "v_mfma_f32_16x16x32_bf16 v[8:11], v[28:31], v[48:51], v[8:11]\n" \
  "ds_read_b128 v[24:27], v113 offset:19456\n" \
  "s_waitcnt  lgkmcnt(3)\n" \
  "v_mfma_f32_16x16x32_bf16 v[12:15], v[32:35], v[48:51], v[12:15]\n" \
  "ds_read_b128 v[28:31], v113 offset:20480\n" \
  "s_waitcnt  lgkmcnt(3)\n" \
  "v_mfma_f32_16x16x32_bf16 v[16:19], v[108:111], v[48:51], v[16:19]\n" \
  "ds_read_b128 v[32:35], v113 offset:21504\n" \
  "s_waitcnt vmcnt(7) lgkmcnt(3)\n" \
  "v_mfma_f32_16x16x32_bf16 v[0:3], v[20:23], v[52:55], v[0:3]\n" \
  "global_load_dwordx4 v[84:87], v112, s[70:71] sc1\n" \
  "ds_read_b128 v[108:111], v122 offset:18432\n" \
  "s_waitcnt  lgkmcnt(3)\n" \
  "v_mfma_f32_16x16x32_bf16 v[4:7], v[24:27], v[52:55], v[4:7]\n" \
  "ds_read_b128 v[20:23], v113 offset:23040\n" \
  "s_waitcnt  lgkmcnt(3)\n" \
  "v_mfma_f32_16x16x32_bf16 v[8:11], v[28:31], v[52:55], v[8:11]\n" \
  "ds_read_b128 v[24:27], v113 offset:24064\n" \
  "s_waitcnt  lgkmcnt(3)\n" \
  "v_mfma_f32_16x16x32_bf16 v[12:15], v[32:35], v[52:55], v[12:15]\n" \
  "ds_read_b128 v[28:31], v113 offset:25088\n" \
  "s_waitcnt  lgkmcnt(3)\n" \
  "v_mfma_f32_16x16x32_bf16 v[16:19], v[108:111], v[52:55], v[16:19]\n" \
  "ds_read_b128 v[32:35], v113 offset:26112\n" \
  "s_waitcnt vmcnt(7) lgkmcnt(3)\n" \
  "v_mfma_f32_16x16x32_bf16 v[0:3], v[20:23], v[56:59], v[0:3]\n" \
  "global_load_dwordx4 v[88:91], v112, s[70:71] offset:1024 sc1\n" \
  "ds_read_b128 v[108:111], v122 offset:23040\n" \
  "s_waitcnt  lgkmcnt(3)\n" \
  "v_mfma_f32_16x16x32_bf16 v[4:7], v[24:27], v[56:59], v[4:7]\n" \
  "ds_read_b128 v[20:23], v113 offset:27648\n" \
  "s_waitcnt  lgkmcnt(3)\n" \
  "v_mfma_f32_16x16x32_bf16 v[8:11], v[28:31], v[56:59], v[8:11]\n" \
  "ds_read_b128 v[24:27], v113 offset:28672\n" \
  "s_waitcnt  lgkmcnt(3)\n" \
  "v_mfma_f32_16x16x32_bf16 v[12:15], v[32:35], v[56:59], v[12:15]\n" \
  "ds_read_b128 v[28:31], v113 offset:29696\n" \
  "s_waitcnt  lgkmcnt(3)\n" \
  "v_mfma_f32_16x16x32_bf16 v[16:19], v[108:111], v[56:59], v[16:19]\n" \
  "ds_read_b128 v[32:35], v113 offset:30720\n" \
  "s_waitcnt vmcnt(7) lgkmcnt(3)\n" \
  "v_mfma_f32_16x16x32_bf16 v[0:3], v[20:23], v[60:63], v[0:3]\n" \
  "global_load_dwordx4 v[92:95], v112, s[70:71] offset:2048 sc1\n" \
  "ds_read_b128 v[108:111], v122 offset:27648\n" \
  "s_waitcnt  lgkmcnt(3)\n" \
  "v_mfma_f32_16x16x32_bf16 v[4:7], v[24:27], v[60:63], v[4:7]\n" \
  "ds_read_b128 v[20:23], v113 offset:32256\n" \
  "s_waitcnt  lgkmcnt(3)\n" \
  "v_mfma_f32_16x16x32_bf16 v[8:11], v[28:31], v[60:63], v[8:11]\n" \
  "ds_read_b128 v[24:27], v113 offset:33280\n" \
  "s_waitcnt  lgkmcnt(3)\n" \
  "v_mfma_f32_16x16x32_bf16 v[12:15], v[32:35], v[60:63], v[12:15]\n" \
  "ds_read_b128 v[28:31], v113 offset:34304\n" \
  "s_waitcnt  lgkmcnt(3)\n" \
  "v_mfma_f32_16x16x32_bf16 v[16:19], v[108:111], v[60:63], v[16:19]\n" \
  "ds_read_b128 v[32:35], v113 offset:35328\n" \
  "s_waitcnt vmcnt(7) lgkmcnt(3)\n" \
  "v_mfma_f32_16x16x32_bf16 v[0:3], v[20:23], v[64:67], v[0:3]\n" \
  "global_load_dwordx4 v[96:99], v112, s[70:71] offset:3072 sc1\n" \
  "ds_read_b128 v[108:111], v122 offset:32256\n" \
  "s_waitcnt  lgkmcnt(3)\n" \
  "v_mfma_f32_16x16x32_bf16 v[4:7], v[24:27], v[64:67], v[4:7]\n" \
  "ds_read_b128 v[20:23], v113 offset:36864\n" \
  "s_waitcnt  lgkmcnt(3)\n" \
  "v_mfma_f32_16x16x32_bf16 v[8:11], v[28:31], v[64:67], v[8:11]\n" \
  "ds_read_b128 v[24:27], v113 offset:37888\n" \
  "s_waitcnt  lgkmcnt(3)\n" \
  "v_mfma_f32_16x16x32_bf16 v[12:15], v[32:35], v[64:67], v[12:15]\n" \
  "ds_read_b128 v[28:31], v113 offset:38912\n" \
  "s_waitcnt  lgkmcnt(3)\n" \
  "v_mfma_f32_16x16x32_bf16 v[16:19], v[108:111], v[64:67], v[16:19]\n" \
  "ds_read_b128 v[32:35], v113 offset:39936\n" \
  "s_waitcnt vmcnt(7) lgkmcnt(3)\n" \
  "v_mfma_f32_16x16x32_bf16 v[0:3], v[20:23], v[68:71], v[0:3]\n" \
  "global_load_dword v100, v126, s[28:29] sc1\n" \
  "global_load_dword v101, v126, s[28:29] offset:256 sc1\n" \
  "global_load_dword v102, v126, s[28:29] offset:512 sc1\n" \
  "global_load_dword v103, v126, s[28:29] offset:768 sc1\n" \
  "ds_read_b128 v[108:111], v122 offset:36864\n" \
  "s_waitcnt  lgkmcnt(3)\n" \
  "v_mfma_f32_16x16x32_bf16 v[4:7], v[24:27], v[68:71], v[4:7]\n" \
  "ds_read_b128 v[20:23], v113 offset:41472\n" \
  "s_waitcnt  lgkmcnt(3)\n" \
  "v_mfma_f32_16x16x32_bf16 v[8:11], v[28:31], v[68:71], v[8:11]\n" \
  "ds_read_b128 v[24:27], v113 offset:42496\n" \
  "s_waitcnt  lgkmcnt(3)\n" \
  "v_mfma_f32_16x16x32_bf16 v[12:15], v[32:35], v[68:71], v[12:15]\n" \
  "ds_read_b128 v[28:31], v113 offset:43520\n" \
  "s_waitcnt  lgkmcnt(3)\n" \
  "v_mfma_f32_16x16x32_bf16 v[16:19], v[108:111], v[68:71], v[16:19]\n" \
  "ds_read_b128 v[32:35], v113 offset:44544\n" \
  "s_waitcnt vmcnt(10) lgkmcnt(3)\n" \
  "v_mfma_f32_16x16x32_bf16 v[0:3], v[20:23], v[72:75], v[0:3]\n" \
  "global_load_dword v104, v126, s[28:29] offset:1024 sc1\n" \
  "global_load_dword v105, v126, s[28:29] offset:1280 sc1\n" \
  "global_load_dword v106, v126, s[28:29] offset:1536 sc1\n" \
  "global_load_dword v107, v126, s[28:29] offset:1792 sc1\n" \
  "ds_read_b128 v[108:111], v122 offset:41472\n" \
  "s_waitcnt  lgkmcnt(3)\n" \
  "v_mfma_f32_16x16x32_bf16 v[4:7], v[24:27], v[72:75], v[4:7]\n" \
  "ds_read_b128 v[20:23], v114\n" \
  "s_waitcnt  lgkmcnt(3)\n" \
  "v_mfma_f32_16x16x32_bf16 v[8:11], v[28:31], v[72:75], v[8:11]\n" \
  "ds_read_b128 v[24:27], v114 offset:1024\n" \
  "s_waitcnt  lgkmcnt(3)\n" \
  "v_mfma_f32_16x16x32_bf16 v[12:15], v[32:35], v[72:75], v[12:15]\n" \
  "ds_read_b128 v[28:31], v114 offset:2048\n" \
  "s_waitcnt  lgkmcnt(3)\n" \
  "v_mfma_f32_16x16x32_bf16 v[16:19], v[108:111], v[72:75], v[16:19]\n" \
  "ds_read_b128 v[32:35], v114 offset:3072\n" \
  "s_waitcnt vmcnt(13) lgkmcnt(3)\n" \
  "v_mfma_f32_16x16x32_bf16 v[0:3], v[20:23], v[76:79], v[0:3]\n" \
  "ds_read_b128 v[108:111], v123\n" \
  "s_waitcnt  lgkmcnt(3)\n" \
  "v_mfma_f32_16x16x32_bf16 v[4:7], v[24:27], v[76:79], v[4:7]\n" \
  "ds_read_b128 v[20:23], v114 offset:4608\n" \
  "s_waitcnt  lgkmcnt(3)\n" \
  "v_mfma_f32_16x16x32_bf16 v[8:11], v[28:31], v[76:79], v[8:11]\n" \
  "ds_read_b128 v[24:27], v114 offset:5632\n" \
  "s_waitcnt  lgkmcnt(3)\n" \
  "v_mfma_f32_16x16x32_bf16 v[12:15], v[32:35], v[76:79], v[12:15]\n" \
  "ds_read_b128 v[28:31], v114 offset:6656\n" \
  "s_waitcnt  lgkmcnt(3)\n" \
  "v_mfma_f32_16x16x32_bf16 v[16:19], v[108:111], v[76:79], v[16:19]\n" \
  "ds_read_b128 v[32:35], v114 offset:7680\n" \
  "s_waitcnt vmcnt(12) lgkmcnt(3)\n" \
  "v_mfma_f32_16x16x32_bf16 v[0:3], v[20:23], v[80:83], v[0:3]\n" \
  "ds_read_b128 v[108:111], v123 offset:4608\n" \
  "s_waitcnt  lgkmcnt(3)\n" \
  "v_mfma_f32_16x16x32_bf16 v[4:7], v[24:27], v[80:83], v[4:7]\n" \
  "ds_read_b128 v[20:23], v114 offset:9216\n" \
  "s_waitcnt  lgkmcnt(3)\n" \
  "v_mfma_f32_16x16x32_bf16 v[8:11], v[28:31], v[80:83], v[8:11]\n" \
  "ds_read_b128 v[24:27], v114 offset:10240\n" \
  "s_waitcnt  lgkmcnt(3)\n" \
  "v_mfma_f32_16x16x32_bf16 v[12:15], v[32:35], v[80:83], v[12:15]\n" \
  "ds_read_b128 v[28:31], v114 offset:11264\n" \
  "s_waitcnt  lgkmcnt(3)\n" \
  "v_mfma_f32_16x16x32_bf16 v[16:19], v[108:111], v[80:83], v[16:19]\n" \
  "ds_read_b128 v[32:35], v114 offset:12288\n" \
  "s_waitcnt vmcnt(11) lgkmcnt(3)\n" \
  "v_mfma_f32_16x16x32_bf16 v[0:3], v[20:23], v[84:87], v[0:3]\n" \
  "ds_read_b128 v[108:111], v123 offset:9216\n" \
  "s_waitcnt  lgkmcnt(3)\n" \
  "v_mfma_f32_16x16x32_bf16 v[4:7], v[24:27], v[84:87], v[4:7]\n" \
  "ds_read_b128 v[20:23], v114 offset:13824\n" \
  "s_waitcnt  lgkmcnt(3)\n" \
  "v_mfma_f32_16x16x32_bf16 v[8:11], v[28:31], v[84:87], v[8:11]\n" \
  "ds_read_b128 v[24:27], v114 offset:14848\n" \
  "s_waitcnt  lgkmcnt(3)\n" \
  "v_mfma_f32_16x16x32_bf16 v[12:15], v[32:35], v[84:87], v[12:15]\n" \
  "ds_read_b128 v[28:31], v114 offset:15872\n" \
  "s_waitcnt  lgkmcnt(3)\n" \
  "v_mfma_f32_16x16x32_bf16 v[16:19], v[108:111], v[84:87], v[16:19]\n" \
  "ds_read_b128 v[32:35], v114 offset:16896\n" \
  "s_waitcnt vmcnt(10) lgkmcnt(3)\n" \
  "v_mfma_f32_16x16x32_bf16 v[0:3], v[20:23], v[88:91], v[0:3]\n" \
  "ds_read_b128 v[108:111], v123 offset:13824\n" \
  "s_waitcnt  lgkmcnt(3)\n" \
  "v_mfma_f32_16x16x32_bf16 v[4:7], v[24:27], v[88:91], v[4:7]\n" \
  "ds_read_b128 v[20:23], v114 offset:18432\n" \
  "s_waitcnt  lgkmcnt(3)\n" \
  "v_mfma_f32_16x16x32_bf16 v[8:11], v[28:31], v[88:91], v[8:11]\n" \
  "ds_read_b128 v[24:27], v114 offset:19456\n" \
  "s_waitcnt  lgkmcnt(3)\n" \
  "v_mfma_f32_16x16x32_bf16 v[12:15], v[32:35], v[88:91], v[12:15]\n" \
  "ds_read_b128 v[28:31], v114 offset:20480\n" \
  "s_waitcnt  lgkmcnt(3)\n" \
  "v_mfma_f32_16x16x32_bf16 v[16:19], v[108:111], v[88:91], v[16:19]\n" \
  "ds_read_b128 v[32:35], v114 offset:21504\n" \
  "s_waitcnt vmcnt(9) lgkmcnt(3)\n" \
  "v_mfma_f32_16x16x32_bf16 v[0:3], v[20:23], v[92:95], v[0:3]\n" \
  "ds_read_b128 v[108:111], v123 offset:18432\n" \
  "s_waitcnt  lgkmcnt(3)\n" \
  "v_mfma_f32_16x16x32_bf16 v[4:7], v[24:27], v[92:95], v[4:7]\n" \
  "ds_read_b128 v[20:23], v114 offset:23040\n" \
  "s_waitcnt  lgkmcnt(3)\n" \
  "v_mfma_f32_16x16x32_bf16 v[8:11], v[28:31], v[92:95], v[8:11]\n" \
  "ds_read_b128 v[24:27], v114 offset:24064\n" \
  "s_waitcnt  lgkmcnt(3)\n" \
  "v_mfma_f32_16x16x32_bf16 v[12:15], v[32:35], v[92:95], v[12:15]\n" \
  "ds_read_b128 v[28:31], v114 offset:25088\n" \
  "s_waitcnt  lgkmcnt(3)\n" \
  "v_mfma_f32_16x16x32_bf16 v[16:19], v[108:111], v[92:95], v[16:19]\n" \
  "ds_read_b128 v[32:35], v114 offset:26112\n" \
  "s_waitcnt vmcnt(8) lgkmcnt(3)\n" \
  "v_mfma_f32_16x16x32_bf16 v[0:3], v[20:23], v[96:99], v[0:3]\n" \
  "ds_read_b128 v[108:111], v123 offset:23040\n" \
  "s_waitcnt  lgkmcnt(3)\n" \
  "v_mfma_f32_16x16x32_bf16 v[4:7], v[24:27], v[96:99], v[4:7]\n" \
  "ds_read_b128 v[20:23], v114 offset:27648\n" \
  "s_waitcnt  lgkmcnt(3)\n" \
  "v_mfma_f32_16x16x32_bf16 v[8:11], v[28:31], v[96:99], v[8:11]\n" \
  "ds_read_b128 v[24:27], v114 offset:28672\n" \
  "s_waitcnt  lgkmcnt(3)\n" \
  "v_mfma_f32_16x16x32_bf16 v[12:15], v[32:35], v[96:99], v[12:15]\n" \
  "ds_read_b128 v[28:31], v114 offset:29696\n" \
  "s_waitcnt  lgkmcnt(3)\n" \
  "v_mfma_f32_16x16x32_bf16 v[16:19], v[108:111], v[96:99], v[16:19]\n" \
  "ds_read_b128 v[32:35], v114 offset:30720\n" \
  "s_waitcnt vmcnt(4) lgkmcnt(3)\n" \
  "v_mfma_f32_16x16x32_bf16 v[0:3], v[20:23], v[100:103], v[0:3]\n" \
  "ds_read_b128 v[108:111], v123 offset:27648\n" \
  "s_waitcnt  lgkmcnt(3)\n" \
  "v_mfma_f32_16x16x32_bf16 v[4:7], v[24:27], v[100:103], v[4:7]\n" \
  "ds_read_b128 v[20:23], v114 offset:32256\n" \
  "s_waitcnt  lgkmcnt(3)\n" \
  "v_mfma_f32_16x16x32_bf16 v[8:11], v[28:31], v[100:103], v[8:11]\n" \
  "ds_read_b128 v[24:27], v114 offset:33280\n" \
  "s_waitcnt  lgkmcnt(3)\n" \
  "v_mfma_f32_16x16x32_bf16 v[12:15], v[32:35], v[100:103], v[12:15]\n" \
  "ds_read_b128 v[28:31], v114 offset:34304\n" \
  "s_waitcnt  lgkmcnt(3)\n" \
  "v_mfma_f32_16x16x32_bf16 v[16:19], v[108:111], v[100:103], v[16:19]\n" \
  "ds_read_b128 v[32:35], v114 offset:35328\n" \
  "s_waitcnt vmcnt(0) lgkmcnt(3)\n" \
  "v_mfma_f32_16x16x32_bf16 v[0:3], v[20:23], v[104:107], v[0:3]\n" \
  "ds_read_b128 v[108:111], v123 offset:32256\n" \
  "s_waitcnt  lgkmcnt(3)\n" \
  "v_mfma_f32_16x16x32_bf16 v[4:7], v[24:27], v[104:107], v[4:7]\n" \
  "s_waitcnt  lgkmcnt(2)\n" \
  "v_mfma_f32_16x16x32_bf16 v[8:11], v[28:31], v[104:107], v[8:11]\n" \
  "s_waitcnt  lgkmcnt(1)\n" \
  "v_mfma_f32_16x16x32_bf16 v[12:15], v[32:35], v[104:107], v[12:15]\n" \
  "s_waitcnt  lgkmcnt(0)\n" \
  "v_mfma_f32_16x16x32_bf16 v[16:19], v[108:111], v[104:107], v[16:19]\n" \
  "v_cvt_pk_bf16_f32 v0, v0, v1\n" \
  "v_cvt_pk_bf16_f32 v1, v2, v3\n" \
  "ds_write_b64 v115, v[0:1]\n" \
  "v_cvt_pk_bf16_f32 v4, v4, v5\n" \
  "v_cvt_pk_bf16_f32 v5, v6, v7\n" \
  "ds_write_b64 v115, v[4:5] offset:32\n" \
  "v_cvt_pk_bf16_f32 v8, v8, v9\n" \
  "v_cvt_pk_bf16_f32 v9, v10, v11\n" \
  "ds_write_b64 v115, v[8:9] offset:64\n" \
  "v_cvt_pk_bf16_f32 v12, v12, v13\n" \
  "v_cvt_pk_bf16_f32 v13, v14, v15\n" \
  "ds_write_b64 v115, v[12:13] offset:96\n" \
  "v_cvt_pk_bf16_f32 v16, v16, v17\n" \
  "v_cvt_pk_bf16_f32 v17, v18, v19\n" \
  "s_mov_b32 exec_hi, 0\n" \
  "ds_write_b64 v115, v[16:17] offset:128\n" \
  "s_mov_b32 exec_hi, -1\n" \
  "s_mov_b64 exec, 0xffff\n" \
  "ds_read_b128 v[28:31], v115 offset:128\n" \
  "s_mov_b64 exec, -1\n" \
  "ds_read_b128 v[20:23], v116\n" \
  "ds_read_b128 v[24:27], v116 offset:1152\n" \
  "s_cmp_eq_u32 s10, 0\n" \
  "s_cbranch_scc1 Lo3_%=\n" \
  "s_branch Lzw57_%=\n" \
  "Lzw56_%=:\n" \
  "s_sleep 1\n" \
  "Lzw57_%=:\n" \
  "ds_read_b32 v3, v121 offset:64\n" \
  "s_waitcnt lgkmcnt(0)\n" \
  "v_readfirstlane_b32 s42, v3\n" \
  "s_bitcmp1_b32 s42, 0\n" \
  "s_cbranch_scc0 Lzw56_%=\n" \
  "s_add_u32 s36, s6, s4\n" \
  "s_cmp_lg_u32 s36, 0\n" \
  "s_cbranch_scc1 Lo2_%=\n" \
  "v_mov_b32 v0, s14\n" \
  "v_mov_b32 v1, s15\n" \
  "s_lshl_b32 s36, s7, 3\n" \
  "v_mov_b32 v2, s36\n" \
  "s_mov_b64 exec, 1\n" \
  "global_atomic_swap_x2 v2, v[0:1], s[24:25] sc1\n" \
  "s_mov_b64 exec, -1\n" \
  "Lo2_%=:\n" \
  "s_sleep 18\n" \
  "Lo3_%=:\n" \
  "s_mov_b64 exec, 0xffff\n" \
  "s_waitcnt lgkmcnt(2)\n" \
  "global_store_dwordx4 v119, v[28:31], s[22:23] sc1\n" \
  "s_mov_b64 exec, -1\n" \
  "s_waitcnt lgkmcnt(1)\n" \
  "global_store_dwordx4 v117, v[20:23], s[22:23] sc1 nt\n" \
  "s_waitcnt lgkmcnt(0)\n" \
  "global_store_dwordx4 v118, v[24:27], s[22:23] sc1 nt\n" \
  "s_endpgm\n" \
  "Lt3_%=:\n" \
  "s_sleep 2\n" \
  "s_swappc_b64 s[70:71], s[70:71]\n" \
  "s_branch Lt4_%=\n" \
    \
  "Lepq_%=:\n" \
  "s_nop 1\n" \
  "s_mov_b32 s36, 0x3eae1a5b\n" \
  "s_mov_b32 s37, s36\n" \
  "v_pk_mul_f32 v[0:1], v[0:1], s[36:37]\n" \
  "v_pk_mul_f32 v[2:3], v[2:3], s[36:37]\n" \
  "v_pk_mul_f32 v[20:21], v[20:21], s[36:37]\n" \
  "v_pk_mul_f32 v[22:23], v[22:23], s[36:37]\n" \
  "v_cvt_pk_bf16_f32 v0, v0, v1\n" \
  "v_cvt_pk_bf16_f32 v1, v2, v3\n" \
  "v_cvt_pk_bf16_f32 v2, v20, v21\n" \
  "v_cvt_pk_bf16_f32 v3, v22, v23\n" \
  "v_pk_mul_f32 v[4:5], v[4:5], s[36:37]\n" \
  "v_pk_mul_f32 v[6:7], v[6:7], s[36:37]\n" \
  "v_pk_mul_f32 v[24:25], v[24:25], s[36:37]\n" \
  "v_pk_mul_f32 v[26:27], v[26:27], s[36:37]\n" \
  "v_cvt_pk_bf16_f32 v4, v4, v5\n" \
  "v_cvt_pk_bf16_f32 v5, v6, v7\n" \
  "v_cvt_pk_bf16_f32 v6, v24, v25\n" \
  "v_cvt_pk_bf16_f32 v7, v26, v27\n" \
  "v_permlane16_swap_b32 v0, v2\n" \
  "v_permlane16_swap_b32 v1, v3\n" \
  "ds_write_b128 v102, v[0:3]\n" \
  "v_pk_mul_f32 v[8:9], v[8:9], s[36:37]\n" \
  "v_pk_mul_f32 v[10:11], v[10:11], s[36:37]\n" \
  "v_pk_mul_f32 v[28:29], v[28:29], s[36:37]\n" \
  "v_pk_mul_f32 v[30:31], v[30:31], s[36:37]\n" \
  "v_cvt_pk_bf16_f32 v8, v8, v9\n" \
  "v_cvt_pk_bf16_f32 v9, v10, v11\n" \
  "v_cvt_pk_bf16_f32 v10, v28, v29\n" \
  "v_cvt_pk_bf16_f32 v11, v30, v31\n" \
  "v_permlane16_swap_b32 v4, v6\n" \
  "v_permlane16_swap_b32 v5, v7\n" \
  "ds_write_b128 v102, v[4:7] offset:1024\n" \
  "v_pk_mul_f32 v[12:13], v[12:13], s[36:37]\n" \
  "v_pk_mul_f32 v[14:15], v[14:15], s[36:37]\n" \
  "v_pk_mul_f32 v[32:33], v[32:33], s[36:37]\n" \
  "v_pk_mul_f32 v[34:35], v[34:35], s[36:37]\n" \
  "v_cvt_pk_bf16_f32 v12, v12, v13\n" \
  "v_cvt_pk_bf16_f32 v13, v14, v15\n" \
  "v_cvt_pk_bf16_f32 v14, v32, v33\n" \
  "v_cvt_pk_bf16_f32 v15, v34, v35\n" \
  "v_permlane16_swap_b32 v8, v10\n" \
  "v_permlane16_swap_b32 v9, v11\n" \
  "ds_write_b128 v102, v[8:11] offset:2048\n" \
  "v_pk_mul_f32 v[16:17], v[16:17], s[36:37]\n" \
  "v_pk_mul_f32 v[18:19], v[18:19], s[36:37]\n" \
  "v_pk_mul_f32 v[36:37], v[36:37], s[36:37]\n" \
  "v_pk_mul_f32 v[38:39], v[38:39], s[36:37]\n" \
  "v_cvt_pk_bf16_f32 v16, v16, v17\n" \
  "v_cvt_pk_bf16_f32 v17, v18, v19\n" \
  "v_cvt_pk_bf16_f32 v18, v36, v37\n" \
  "v_cvt_pk_bf16_f32 v19, v38, v39\n" \
  "v_permlane16_swap_b32 v12, v14\n" \
  "v_permlane16_swap_b32 v13, v15\n" \
  "ds_write_b128 v102, v[12:15] offset:3072\n" \
  "s_nop 1\n" \
  "v_permlane16_swap_b32 v16, v18\n" \
  "v_permlane16_swap_b32 v17, v19\n" \
  "ds_write_b128 v104, v[16:19] offset:4096\n" \
    \
  "Lattn_%=:\n" \
  "s_mov_b64 exec, 1\n" \
  "ds_add_u32 v121, v122 offset:40\n" \
  "s_mov_b64 exec, -1\n" \
  "Lattn2_%=:\n" \
  "v_mov_b64 v[98:99], 0\n" \
  "v_mov_b64 v[100:101], 0\n" \
  "v_mov_b64 v[102:103], 0\n" \
  "v_mov_b64 v[104:105], 0\n" \
  "s_mov_b32 exec_lo, 0x11000000\n" \
  "s_mov_b32 exec_hi, 0x11000000\n" \
  "v_mov_b32 v98, 0x3f803f80\n" \
  "v_mov_b32 v99, 0x3f803f80\n" \
  "v_mov_b64 v[100:101], v[98:99]\n" \
  "v_mov_b64 v[102:103], v[98:99]\n" \
  "v_mov_b64 v[104:105], v[98:99]\n" \
  "s_mov_b64 exec, -1\n" \
  "s_add_u32 s68, s28, 36864\n" \
  "s_addc_u32 s69, s29, 0\n" \
  "s_cmp_lg_u32 s10, 0\n" \
  "s_cbranch_scc1 Lat0_%=\n" \
  "s_branch Lzw59_%=\n" \
  "Lzw58_%=:\n" \
  "s_sleep 1\n" \
  "Lzw59_%=:\n" \
  "ds_read_b32 v113, v121 offset:40\n" \
  "s_waitcnt lgkmcnt(0)\n" \
  "v_readfirstlane_b32 s66, v113\n" \
  "s_bitcmp1_b32 s66, 4\n" \
  "s_cbranch_scc0 Lzw58_%=\n" \
  "Lat0_%=:\n" \
  "s_setprio 1\n" \
  "s_cmp_eq_u32 s10, 0\n" \
  "s_cbranch_scc1 Lat1_%=\n" \
  "s_setprio 2\n" \
  "Lat1_%=:\n" \
    \
  "Lat2_%=:\n" \
  "ds_read_b32 v113, v121 offset:40\n" \
  "ds_read_b64 v[88:89], v107 offset:4608\n" \
  "s_mov_b32 exec_hi, 0\n" \
  "ds_read2st64_b64 v[116:119], v115 offset0:81 offset1:72\n" \
  "s_mov_b32 exec_hi, -1\n" \
  "ds_read_b128 v[80:83], v106 offset:4608\n" \
  "ds_read_b128 v[16:19], v114 offset:41472\n" \
  "ds_read_b64 v[90:91], v107\n" \
  "ds_read_b128 v[84:87], v106\n" \
  "ds_read_b128 v[92:95], v114 offset:36864\n" \
  "s_waitcnt lgkmcnt(7)\n" \
  "v_readfirstlane_b32 s66, v113\n" \
  "s_bitcmp1_b32 s66, 4\n" \
  "s_cbranch_scc0 Lat2_%=\n" \
  "s_waitcnt lgkmcnt(5)\n" \
  "v_and_b32 v88, v88, v108\n" \
  "v_and_b32 v89, v89, v109\n" \
  "s_nop 0\n" \
  "s_waitcnt lgkmcnt(2)\n" \
  "v_mfma_f32_32x32x8_bf16 v[32:47], v[116:117], v[88:89], v[64:79]\n" \
  "v_and_b32 v90, v90, v108\n" \
  "v_and_b32 v91, v91, v109\n" \
  "v_mfma_f32_32x32x16_bf16 v[32:47], v[16:19], v[80:83], v[32:47]\n" \
  "s_mov_b64 exec, s[86:87]\n" \
  "ds_read_b128 v[98:101], v96\n" \
  "ds_read_b128 v[102:105], v97\n" \
  "s_mov_b64 exec, -1\n" \
  "v_mfma_f32_32x32x8_bf16 v[48:63], v[118:119], v[90:91], v[64:79]\n" \
  "s_waitcnt lgkmcnt(2)\n" \
  "v_mfma_f32_32x32x16_bf16 v[48:63], v[92:95], v[84:87], v[48:63]\n" \
  "s_nop 4\n" \
  "v_max3_f32 v113, v32, v33, v34\n" \
  "v_max3_f32 v120, v35, v36, v37\n" \
  "v_max3_f32 v113, v113, v38, v39\n" \
  "v_max3_f32 v120, v120, v40, v41\n" \
  "v_max3_f32 v113, v113, v42, v43\n" \
  "v_max3_f32 v120, v120, v44, v45\n" \
  "v_max3_f32 v113, v113, v46, v47\n" \
  "v_max_f32 v32, v113, v120\n" \
  "v_max_f32 v113, v113, v120\n" \
  "s_nop 1\n" \
  "v_permlane32_swap_b32 v113, v32\n" \
  "s_mov_b32 exec_lo, 0\n" \
  "v_max_f32 v113, v113, v32\n" \
  "v_cvt_pk_bf16_f32 v88, -v113, 0\n" \
  "s_mov_b32 exec_lo, -1\n" \
  "s_nop 0\n" \
  "v_mfma_f32_32x32x8_bf16 v[32:47], v[116:117], v[88:89], v[64:79]\n" \
  "v_mfma_f32_32x32x16_bf16 v[32:47], v[16:19], v[80:83], v[32:47]\n" \
  "v_max3_f32 v113, v48, v49, v50\n" \
  "v_max3_f32 v120, v51, v52, v53\n" \
  "v_max3_f32 v113, v113, v54, v55\n" \
  "v_max3_f32 v120, v120, v56, v57\n" \
  "v_max3_f32 v113, v113, v58, v59\n" \
  "v_max3_f32 v120, v120, v60, v61\n" \
  "v_max3_f32 v113, v113, v62, v63\n" \
  "v_max_f32 v48, v113, v120\n" \
  "v_max_f32 v113, v113, v120\n" \
  "s_nop 1\n" \
  "v_permlane32_swap_b32 v113, v48\n" \
  "s_mov_b32 exec_lo, 0\n" \
  "v_max_f32 v113, v113, v48\n" \
  "v_cvt_pk_bf16_f32 v90, -v113, 0\n" \
  "s_mov_b32 exec_lo, -1\n" \
  "v_exp_f32 v32, v32\n" \
  "v_exp_f32 v33, v33\n" \
    \
  "La0_%=:\n" \
  "v_exp_f32 v34, v34\n" \
  "v_cvt_pk_bf16_f32 v106, v32, v33\n" \
  "v_exp_f32 v35, v35\n" \
  "v_exp_f32 v36, v36\n" \
  "v_cvt_pk_bf16_f32 v107, v34, v35\n" \
  "v_exp_f32 v37, v37\n" \
  "v_mfma_f32_32x32x8_bf16 v[48:63], v[118:119], v[88:89], 0\n" \
  "v_exp_f32 v38, v38\n" \
  "v_cvt_pk_bf16_f32 v108, v36, v37\n" \
  "v_exp_f32 v39, v39\n" \
  "v_exp_f32 v40, v40\n" \
  "v_cvt_pk_bf16_f32 v109, v38, v39\n" \
  "v_exp_f32 v41, v41\n" \
  "v_mfma_f32_32x32x16_bf16 v[48:63], v[92:95], v[80:83], v[48:63]\n" \
  "s_waitcnt lgkmcnt(0)\n" \
  "v_exp_f32 v42, v42\n" \
  "v_cvt_pk_bf16_f32 v110, v40, v41\n" \
  "v_exp_f32 v43, v43\n" \
  "v_exp_f32 v44, v44\n" \
  "v_exp_f32 v45, v45\n" \
  "v_cvt_pk_bf16_f32 v111, v42, v43\n" \
  "v_mfma_f32_32x32x16_bf16 v[0:15], v[98:101], v[106:109], 0\n" \
  "s_bfe_u32 s67, s60, 0x80000\n" \
  "v_exp_f32 v46, v46\n" \
  "v_exp_f32 v47, v47\n" \
  "v_cvt_pk_bf16_f32 v112, v44, v45\n" \
  "v_cvt_pk_bf16_f32 v113, v46, v47\n" \
  "v_exp_f32 v48, v48\n" \
  "v_exp_f32 v49, v49\n" \
  "v_mfma_f32_32x32x16_bf16 v[0:15], v[102:105], v[110:113], v[0:15]\n" \
  "s_mov_b64 exec, s[86:87]\n" \
  "ds_read_b128 v[98:101], v96 offset:4608\n" \
  "ds_read_b128 v[102:105], v97 offset:4608\n" \
  "ds_read_b32 v120, v121\n" \
  "s_mov_b64 exec, -1\n" \
  "La1_%=:\n" \
  "v_exp_f32 v50, v50\n" \
  "v_cvt_pk_bf16_f32 v106, v48, v49\n" \
  "v_exp_f32 v51, v51\n" \
  "v_exp_f32 v52, v52\n" \
  "v_cvt_pk_bf16_f32 v107, v50, v51\n" \
  "v_exp_f32 v53, v53\n" \
  "v_mfma_f32_32x32x8_bf16 v[32:47], v[118:119], v[90:91], v[64:79]\n" \
  "v_exp_f32 v54, v54\n" \
  "v_cvt_pk_bf16_f32 v108, v52, v53\n" \
  "v_exp_f32 v55, v55\n" \
  "v_exp_f32 v56, v56\n" \
  "v_cvt_pk_bf16_f32 v109, v54, v55\n" \
  "v_exp_f32 v57, v57\n" \
  "v_mfma_f32_32x32x16_bf16 v[32:47], v[92:95], v[84:87], v[32:47]\n" \
  "s_waitcnt lgkmcnt(0)\n" \
  "v_readfirstlane_b32 s66, v120\n" \
  "s_mov_b64 exec, s[62:63]\n" \
  "ds_add_u32 v121, v122 offset:4\n" \
  "s_mov_b64 exec, -1\n" \
  "s_bitcmp1_b32 s66, s67\n" \
  "s_cbranch_scc0 Lalt0_%=\n" \
  "s_mov_b32 exec_hi, 0\n" \
  "ds_read2st64_b64 v[116:119], v115 offset0:63 offset1:54\n" \
  "s_mov_b32 exec_hi, -1\n" \
  "ds_read_b128 v[92:95], v114 offset:32256\n" \
  "v_exp_f32 v58, v58\n" \
  "v_cvt_pk_bf16_f32 v110, v56, v57\n" \
  "v_exp_f32 v59, v59\n" \
  "v_exp_f32 v60, v60\n" \
  "v_exp_f32 v61, v61\n" \
  "v_cvt_pk_bf16_f32 v111, v58, v59\n" \
  "v_mfma_f32_32x32x16_bf16 v[0:15], v[98:101], v[106:109], v[0:15]\n" \
  "v_exp_f32 v62, v62\n" \
  "v_exp_f32 v63, v63\n" \
  "v_cvt_pk_bf16_f32 v112, v60, v61\n" \
  "v_cvt_pk_bf16_f32 v113, v62, v63\n" \
  "v_exp_f32 v32, v32\n" \
  "v_exp_f32 v33, v33\n" \
  "v_mfma_f32_32x32x16_bf16 v[0:15], v[102:105], v[110:113], v[0:15]\n" \
  "s_waitcnt lgkmcnt(0)\n" \
  "v_exp_f32 v34, v34\n" \
  "v_cvt_pk_bf16_f32 v106, v32, v33\n" \
  "v_exp_f32 v35, v35\n" \
  "v_exp_f32 v36, v36\n" \
  "v_cvt_pk_bf16_f32 v107, v34, v35\n" \
  "v_exp_f32 v37, v37\n" \
  "v_mfma_f32_32x32x8_bf16 v[48:63], v[116:117], v[88:89], 0\n" \
  "v_exp_f32 v38, v38\n" \
  "v_cvt_pk_bf16_f32 v108, v36, v37\n" \
  "v_exp_f32 v39, v39\n" \
  "v_exp_f32 v40, v40\n" \
  "v_cvt_pk_bf16_f32 v109, v38, v39\n" \
  "v_mfma_f32_32x32x16_bf16 v[48:63], v[92:95], v[80:83], v[48:63]\n" \
  "v_exp_f32 v41, v41\n" \
  "v_exp_f32 v42, v42\n" \
  "v_cvt_pk_bf16_f32 v110, v40, v41\n" \
  "v_exp_f32 v43, v43\n" \
  "v_exp_f32 v44, v44\n" \
  "v_exp_f32 v45, v45\n" \
  "v_cvt_pk_bf16_f32 v111, v42, v43\n" \
  "v_mfma_f32_32x32x16_bf16 v[16:31], v[98:101], v[106:109], 0\n" \
  "v_exp_f32 v46, v46\n" \
  "v_exp_f32 v47, v47\n" \
  "v_cvt_pk_bf16_f32 v112, v44, v45\n" \
  "v_cvt_pk_bf16_f32 v113, v46, v47\n" \
  "v_exp_f32 v48, v48\n" \
  "v_exp_f32 v49, v49\n" \
  "v_mfma_f32_32x32x16_bf16 v[16:31], v[102:105], v[110:113], v[16:31]\n" \
  "s_mov_b64 exec, s[86:87]\n" \
  "ds_read_b128 v[98:101], v96 offset:9216\n" \
  "ds_read_b128 v[102:105], v97 offset:9216\n" \
  "s_mov_b64 exec, -1\n" \
  "La2_%=:\n" \
  "v_exp_f32 v50, v50\n" \
  "v_cvt_pk_bf16_f32 v106, v48, v49\n" \
  "v_exp_f32 v51, v51\n" \
  "v_exp_f32 v52, v52\n" \
  "v_cvt_pk_bf16_f32 v107, v50, v51\n" \
  "v_exp_f32 v53, v53\n" \
  "v_mfma_f32_32x32x8_bf16 v[32:47], v[116:117], v[90:91], 0\n" \
  "v_exp_f32 v54, v54\n" \
  "v_cvt_pk_bf16_f32 v108, v52, v53\n" \
  "v_exp_f32 v55, v55\n" \
  "v_exp_f32 v56, v56\n" \
  "v_cvt_pk_bf16_f32 v109, v54, v55\n" \
  "v_exp_f32 v57, v57\n" \
  "v_mfma_f32_32x32x16_bf16 v[32:47], v[92:95], v[84:87], v[32:47]\n" \
  "s_waitcnt lgkmcnt(0)\n" \
  "s_bitcmp1_b32 s66, s67\n" \
  "s_cbranch_scc0 Lhf2_%=\n" \
  "Lhg2_%=:\n" \
  "ds_read_b128 v[92:95], v114 offset:27648\n" \
  "v_exp_f32 v58, v58\n" \
  "v_cvt_pk_bf16_f32 v110, v56, v57\n" \
  "v_exp_f32 v59, v59\n" \
  "v_exp_f32 v60, v60\n" \
  "v_exp_f32 v61, v61\n" \
  "v_cvt_pk_bf16_f32 v111, v58, v59\n" \
  "v_mfma_f32_32x32x16_bf16 v[0:15], v[98:101], v[106:109], v[0:15]\n" \
  "s_bfe_u32 s67, s60, 0x80008\n" \
  "v_exp_f32 v62, v62\n" \
  "v_exp_f32 v63, v63\n" \
  "v_cvt_pk_bf16_f32 v112, v60, v61\n" \
  "v_cvt_pk_bf16_f32 v113, v62, v63\n" \
  "v_exp_f32 v32, v32\n" \
  "v_exp_f32 v33, v33\n" \
  "v_mfma_f32_32x32x16_bf16 v[0:15], v[102:105], v[110:113], v[0:15]\n" \
  "s_waitcnt lgkmcnt(0)\n" \
  "v_exp_f32 v34, v34\n" \
  "v_cvt_pk_bf16_f32 v106, v32, v33\n" \
  "v_exp_f32 v35, v35\n" \
  "v_exp_f32 v36, v36\n" \
  "v_cvt_pk_bf16_f32 v107, v34, v35\n" \
  "v_exp_f32 v37, v37\n" \
  "v_mfma_f32_32x32x8_bf16 v[48:63], v[118:119], v[88:89], 0\n" \
  "v_exp_f32 v38, v38\n" \
  "v_cvt_pk_bf16_f32 v108, v36, v37\n" \
  "v_exp_f32 v39, v39\n" \
  "v_exp_f32 v40, v40\n" \
  "v_cvt_pk_bf16_f32 v109, v38, v39\n" \
  "v_mfma_f32_32x32x16_bf16 v[48:63], v[92:95], v[80:83], v[48:63]\n" \
  "v_exp_f32 v41, v41\n" \
  "v_exp_f32 v42, v42\n" \
  "v_cvt_pk_bf16_f32 v110, v40, v41\n" \
  "v_exp_f32 v43, v43\n" \
  "v_exp_f32 v44, v44\n" \
  "v_exp_f32 v45, v45\n" \
  "v_cvt_pk_bf16_f32 v111, v42, v43\n" \
  "v_mfma_f32_32x32x16_bf16 v[16:31], v[98:101], v[106:109], v[16:31]\n" \
  "v_exp_f32 v46, v46\n" \
  "v_exp_f32 v47, v47\n" \
  "v_cvt_pk_bf16_f32 v112, v44, v45\n" \
  "v_cvt_pk_bf16_f32 v113, v46, v47\n" \
  "v_exp_f32 v48, v48\n" \
  "v_exp_f32 v49, v49\n" \
  "v_mfma_f32_32x32x16_bf16 v[16:31], v[102:105], v[110:113], v[16:31]\n" \
  "s_mov_b64 exec, s[86:87]\n" \
  "ds_read_b128 v[98:101], v96 offset:13824\n" \
  "ds_read_b128 v[102:105], v97 offset:13824\n" \
  "ds_read_b32 v120, v121\n" \
  "s_mov_b64 exec, -1\n" \
  "La3_%=:\n" \
  "v_exp_f32 v50, v50\n" \
  "v_cvt_pk_bf16_f32 v106, v48, v49\n" \
  "v_exp_f32 v51, v51\n" \
  "v_exp_f32 v52, v52\n" \
  "v_cvt_pk_bf16_f32 v107, v50, v51\n" \
  "v_exp_f32 v53, v53\n" \
  "v_mfma_f32_32x32x8_bf16 v[32:47], v[118:119], v[90:91], 0\n" \
  "v_exp_f32 v54, v54\n" \
  "v_cvt_pk_bf16_f32 v108, v52, v53\n" \
  "v_exp_f32 v55, v55\n" \
  "v_exp_f32 v56, v56\n" \
  "v_cvt_pk_bf16_f32 v109, v54, v55\n" \
  "v_exp_f32 v57, v57\n" \
  "v_mfma_f32_32x32x16_bf16 v[32:47], v[92:95], v[84:87], v[32:47]\n" \
  "s_waitcnt lgkmcnt(0)\n" \
  "v_readfirstlane_b32 s66, v120\n" \
  "s_mov_b64 exec, s[64:65]\n" \
  "ds_add_u32 v121, v122 offset:4\n" \
  "s_mov_b64 exec, -1\n" \
  "s_bitcmp1_b32 s66, s67\n" \
  "s_cbranch_scc0 Lalt1_%=\n" \
  "s_mov_b32 exec_hi, 0\n" \
  "ds_read2st64_b64 v[116:119], v115 offset0:45 offset1:36\n" \
  "s_mov_b32 exec_hi, -1\n" \
  "ds_read_b128 v[92:95], v114 offset:23040\n" \
  "v_exp_f32 v58, v58\n" \
  "v_cvt_pk_bf16_f32 v110, v56, v57\n" \
  "v_exp_f32 v59, v59\n" \
  "v_exp_f32 v60, v60\n" \
  "v_exp_f32 v61, v61\n" \
  "v_cvt_pk_bf16_f32 v111, v58, v59\n" \
  "v_mfma_f32_32x32x16_bf16 v[0:15], v[98:101], v[106:109], v[0:15]\n" \
  "v_exp_f32 v62, v62\n" \
  "v_exp_f32 v63, v63\n" \
  "v_cvt_pk_bf16_f32 v112, v60, v61\n" \
  "v_cvt_pk_bf16_f32 v113, v62, v63\n" \
  "v_exp_f32 v32, v32\n" \
  "v_exp_f32 v33, v33\n" \
  "v_mfma_f32_32x32x16_bf16 v[0:15], v[102:105], v[110:113], v[0:15]\n" \
  "s_waitcnt lgkmcnt(0)\n" \
  "v_exp_f32 v34, v34\n" \
  "v_cvt_pk_bf16_f32 v106, v32, v33\n" \
  "v_exp_f32 v35, v35\n" \
  "v_exp_f32 v36, v36\n" \
  "v_cvt_pk_bf16_f32 v107, v34, v35\n" \
  "v_exp_f32 v37, v37\n" \
  "v_mfma_f32_32x32x8_bf16 v[48:63], v[116:117], v[88:89], 0\n" \
  "v_exp_f32 v38, v38\n" \
  "v_cvt_pk_bf16_f32 v108, v36, v37\n" \
  "v_exp_f32 v39, v39\n" \
  "v_exp_f32 v40, v40\n" \
  "v_cvt_pk_bf16_f32 v109, v38, v39\n" \
  "v_mfma_f32_32x32x16_bf16 v[48:63], v[92:95], v[80:83], v[48:63]\n" \
  "v_exp_f32 v41, v41\n" \
  "v_exp_f32 v42, v42\n" \
  "v_cvt_pk_bf16_f32 v110, v40, v41\n" \
  "v_exp_f32 v43, v43\n" \
  "v_exp_f32 v44, v44\n" \
  "v_exp_f32 v45, v45\n" \
  "v_cvt_pk_bf16_f32 v111, v42, v43\n" \
  "v_mfma_f32_32x32x16_bf16 v[16:31], v[98:101], v[106:109], v[16:31]\n" \
  "v_exp_f32 v46, v46\n" \
  "v_exp_f32 v47, v47\n" \
  "v_cvt_pk_bf16_f32 v112, v44, v45\n" \
  "v_cvt_pk_bf16_f32 v113, v46, v47\n" \
  "v_exp_f32 v48, v48\n" \
  "v_exp_f32 v49, v49\n" \
  "v_mfma_f32_32x32x16_bf16 v[16:31], v[102:105], v[110:113], v[16:31]\n" \
  "s_mov_b64 exec, s[86:87]\n" \
  "ds_read_b128 v[98:101], v96 offset:18432\n" \
  "ds_read_b128 v[102:105], v97 offset:18432\n" \
  "s_mov_b64 exec, -1\n" \
  "La4_%=:\n" \
  "v_exp_f32 v50, v50\n" \
  "v_cvt_pk_bf16_f32 v106, v48, v49\n" \
  "v_exp_f32 v51, v51\n" \
  "v_exp_f32 v52, v52\n" \
  "v_cvt_pk_bf16_f32 v107, v50, v51\n" \
  "v_exp_f32 v53, v53\n" \
  "v_mfma_f32_32x32x8_bf16 v[32:47], v[116:117], v[90:91], 0\n" \
  "v_exp_f32 v54, v54\n" \
  "v_cvt_pk_bf16_f32 v108, v52, v53\n" \
  "v_exp_f32 v55, v55\n" \
  "v_exp_f32 v56, v56\n" \
  "v_cvt_pk_bf16_f32 v109, v54, v55\n" \
  "v_exp_f32 v57, v57\n" \
  "v_mfma_f32_32x32x16_bf16 v[32:47], v[92:95], v[84:87], v[32:47]\n" \
  "s_waitcnt lgkmcnt(0)\n" \
  "s_bitcmp1_b32 s66, s67\n" \
  "s_cbranch_scc0 Lhf4_%=\n" \
  "Lhg4_%=:\n" \
  "ds_read_b128 v[64:67], v123 offset:1024\n" \
  "ds_read_b128 v[68:71], v123 offset:1056\n" \
  "ds_read_b128 v[72:75], v123 offset:1088\n" \
  "ds_read_b128 v[76:79], v123 offset:1120\n" \
  "ds_read_b128 v[92:95], v114 offset:18432\n" \
  "v_exp_f32 v58, v58\n" \
  "v_cvt_pk_bf16_f32 v110, v56, v57\n" \
  "v_exp_f32 v59, v59\n" \
  "v_exp_f32 v60, v60\n" \
  "v_exp_f32 v61, v61\n" \
  "v_cvt_pk_bf16_f32 v111, v58, v59\n" \
  "v_mfma_f32_32x32x16_bf16 v[0:15], v[98:101], v[106:109], v[0:15]\n" \
  "s_bfe_u32 s67, s60, 0x80010\n" \
  "v_exp_f32 v62, v62\n" \
  "v_exp_f32 v63, v63\n" \
  "v_cvt_pk_bf16_f32 v112, v60, v61\n" \
  "v_cvt_pk_bf16_f32 v113, v62, v63\n" \
  "v_exp_f32 v32, v32\n" \
  "v_exp_f32 v33, v33\n" \
  "v_mfma_f32_32x32x16_bf16 v[0:15], v[102:105], v[110:113], v[0:15]\n" \
  "s_waitcnt lgkmcnt(0)\n" \
  "v_exp_f32 v34, v34\n" \
  "v_cvt_pk_bf16_f32 v106, v32, v33\n" \
  "v_exp_f32 v35, v35\n" \
  "v_exp_f32 v36, v36\n" \
  "v_cvt_pk_bf16_f32 v107, v34, v35\n" \
  "v_exp_f32 v37, v37\n" \
  "v_mfma_f32_32x32x8_bf16 v[48:63], v[118:119], v[88:89], 0\n" \
  "v_exp_f32 v38, v38\n" \
  "v_cvt_pk_bf16_f32 v108, v36, v37\n" \
  "v_exp_f32 v39, v39\n" \
  "v_exp_f32 v40, v40\n" \
  "v_cvt_pk_bf16_f32 v109, v38, v39\n" \
  "v_mfma_f32_32x32x16_bf16 v[48:63], v[92:95], v[80:83], v[48:63]\n" \
  "v_exp_f32 v41, v41\n" \
  "v_exp_f32 v42, v42\n" \
  "v_cvt_pk_bf16_f32 v110, v40, v41\n" \
  "v_exp_f32 v43, v43\n" \
  "v_exp_f32 v44, v44\n" \
  "v_exp_f32 v45, v45\n" \
  "v_cvt_pk_bf16_f32 v111, v42, v43\n" \
  "v_mfma_f32_32x32x16_bf16 v[16:31], v[98:101], v[106:109], v[16:31]\n" \
  "v_exp_f32 v46, v46\n" \
  "v_exp_f32 v47, v47\n" \
  "v_cvt_pk_bf16_f32 v112, v44, v45\n" \
  "v_cvt_pk_bf16_f32 v113, v46, v47\n" \
  "v_exp_f32 v48, v48\n" \
  "v_exp_f32 v49, v49\n" \
  "v_mfma_f32_32x32x16_bf16 v[16:31], v[102:105], v[110:113], v[16:31]\n" \
  "s_mov_b64 exec, s[86:87]\n" \
  "ds_read_b128 v[98:101], v96 offset:23040\n" \
  "ds_read_b128 v[102:105], v97 offset:23040\n" \
  "ds_read_b32 v120, v121\n" \
  "s_mov_b64 exec, -1\n" \
  "La5_%=:\n" \
  "v_exp_f32 v50, v50\n" \
  "v_cvt_pk_bf16_f32 v106, v48, v49\n" \
  "v_exp_f32 v51, v51\n" \
  "v_exp_f32 v52, v52\n" \
  "v_cvt_pk_bf16_f32 v107, v50, v51\n" \
  "v_exp_f32 v53, v53\n" \
  "v_mfma_f32_32x32x8_bf16 v[32:47], v[118:119], v[90:91], 0\n" \
  "v_exp_f32 v54, v54\n" \
  "v_cvt_pk_bf16_f32 v108, v52, v53\n" \
  "v_exp_f32 v55, v55\n" \
  "v_exp_f32 v56, v56\n" \
  "v_cvt_pk_bf16_f32 v109, v54, v55\n" \
  "v_exp_f32 v57, v57\n" \
  "v_mfma_f32_32x32x16_bf16 v[32:47], v[92:95], v[84:87], v[32:47]\n" \
  "s_waitcnt lgkmcnt(0)\n" \
  "v_readfirstlane_b32 s66, v120\n" \
  "s_bitcmp1_b32 s66, s67\n" \
  "s_cbranch_scc0 Lalt2_%=\n" \
  "s_mov_b32 exec_hi, 0\n" \
  "ds_read2st64_b64 v[116:119], v115 offset0:27 offset1:18\n" \
  "s_mov_b32 exec_hi, -1\n" \
  "ds_read_b128 v[92:95], v114 offset:13824\n" \
  "v_exp_f32 v58, v58\n" \
  "v_cvt_pk_bf16_f32 v110, v56, v57\n" \
  "v_exp_f32 v59, v59\n" \
  "v_exp_f32 v60, v60\n" \
  "v_exp_f32 v61, v61\n" \
  "v_cvt_pk_bf16_f32 v111, v58, v59\n" \
  "v_mfma_f32_32x32x16_bf16 v[0:15], v[98:101], v[106:109], v[0:15]\n" \
  "v_exp_f32 v62, v62\n" \
  "v_exp_f32 v63, v63\n" \
  "v_cvt_pk_bf16_f32 v112, v60, v61\n" \
  "v_cvt_pk_bf16_f32 v113, v62, v63\n" \
  "v_exp_f32 v32, v32\n" \
  "v_exp_f32 v33, v33\n" \
  "v_mfma_f32_32x32x16_bf16 v[0:15], v[102:105], v[110:113], v[0:15]\n" \
  "s_waitcnt lgkmcnt(0)\n" \
  "v_exp_f32 v34, v34\n" \
  "v_cvt_pk_bf16_f32 v106, v32, v33\n" \
  "v_exp_f32 v35, v35\n" \
  "v_exp_f32 v36, v36\n" \
  "v_cvt_pk_bf16_f32 v107, v34, v35\n" \
  "v_exp_f32 v37, v37\n" \
  "v_mfma_f32_32x32x8_bf16 v[48:63], v[116:117], v[88:89], 0\n" \
  "v_exp_f32 v38, v38\n" \
  "v_cvt_pk_bf16_f32 v108, v36, v37\n" \
  "v_exp_f32 v39, v39\n" \
  "v_exp_f32 v40, v40\n" \
  "v_cvt_pk_bf16_f32 v109, v38, v39\n" \
  "v_mfma_f32_32x32x16_bf16 v[48:63], v[92:95], v[80:83], v[48:63]\n" \
  "v_exp_f32 v41, v41\n" \
  "v_exp_f32 v42, v42\n" \
  "v_cvt_pk_bf16_f32 v110, v40, v41\n" \
  "v_exp_f32 v43, v43\n" \
  "v_exp_f32 v44, v44\n" \
  "v_exp_f32 v45, v45\n" \
  "v_cvt_pk_bf16_f32 v111, v42, v43\n" \
  "v_mfma_f32_32x32x16_bf16 v[16:31], v[98:101], v[106:109], v[16:31]\n" \
  "v_exp_f32 v46, v46\n" \
  "v_exp_f32 v47, v47\n" \
  "v_cvt_pk_bf16_f32 v112, v44, v45\n" \
  "v_cvt_pk_bf16_f32 v113, v46, v47\n" \
  "v_exp_f32 v48, v48\n" \
  "v_exp_f32 v49, v49\n" \
  "v_mfma_f32_32x32x16_bf16 v[16:31], v[102:105], v[110:113], v[16:31]\n" \
  "s_mov_b64 exec, s[86:87]\n" \
  "ds_read_b128 v[98:101], v96 offset:27648\n" \
  "ds_read_b128 v[102:105], v97 offset:27648\n" \
  "s_mov_b64 exec, -1\n" \
  "La6_%=:\n" \
  "v_exp_f32 v50, v50\n" \
  "v_cvt_pk_bf16_f32 v106, v48, v49\n" \
  "v_exp_f32 v51, v51\n" \
  "v_exp_f32 v52, v52\n" \
  "v_cvt_pk_bf16_f32 v107, v50, v51\n" \
  "v_exp_f32 v53, v53\n" \
  "v_mfma_f32_32x32x8_bf16 v[32:47], v[116:117], v[90:91], 0\n" \
  "v_exp_f32 v54, v54\n" \
  "v_cvt_pk_bf16_f32 v108, v52, v53\n" \
  "v_exp_f32 v55, v55\n" \
  "v_exp_f32 v56, v56\n" \
  "v_cvt_pk_bf16_f32 v109, v54, v55\n" \
  "v_exp_f32 v57, v57\n" \
  "v_mfma_f32_32x32x16_bf16 v[32:47], v[92:95], v[84:87], v[32:47]\n" \
  "s_waitcnt lgkmcnt(0)\n" \
  "ds_read_b128 v[92:95], v114 offset:9216\n" \
  "v_exp_f32 v58, v58\n" \
  "v_cvt_pk_bf16_f32 v110, v56, v57\n" \
  "v_exp_f32 v59, v59\n" \
  "v_exp_f32 v60, v60\n" \
  "v_exp_f32 v61, v61\n" \
  "v_cvt_pk_bf16_f32 v111, v58, v59\n" \
  "v_mfma_f32_32x32x16_bf16 v[0:15], v[98:101], v[106:109], v[0:15]\n" \
  "s_bfe_u32 s67, s60, 0x80018\n" \
  "v_exp_f32 v62, v62\n" \
  "v_exp_f32 v63, v63\n" \
  "v_cvt_pk_bf16_f32 v112, v60, v61\n" \
  "v_cvt_pk_bf16_f32 v113, v62, v63\n" \
  "v_exp_f32 v32, v32\n" \
  "v_exp_f32 v33, v33\n" \
  "v_mfma_f32_32x32x16_bf16 v[0:15], v[102:105], v[110:113], v[0:15]\n" \
  "s_waitcnt lgkmcnt(0)\n" \
  "v_exp_f32 v34, v34\n" \
  "v_cvt_pk_bf16_f32 v106, v32, v33\n" \
  "v_exp_f32 v35, v35\n" \
  "v_exp_f32 v36, v36\n" \
  "v_cvt_pk_bf16_f32 v107, v34, v35\n" \
  "v_exp_f32 v37, v37\n" \
  "v_mfma_f32_32x32x8_bf16 v[48:63], v[118:119], v[88:89], 0\n" \
  "v_exp_f32 v38, v38\n" \
  "v_cvt_pk_bf16_f32 v108, v36, v37\n" \
  "v_exp_f32 v39, v39\n" \
  "v_exp_f32 v40, v40\n" \
  "v_cvt_pk_bf16_f32 v109, v38, v39\n" \
  "v_mfma_f32_32x32x16_bf16 v[48:63], v[92:95], v[80:83], v[48:63]\n" \
  "v_exp_f32 v41, v41\n" \
  "v_exp_f32 v42, v42\n" \
  "v_cvt_pk_bf16_f32 v110, v40, v41\n" \
  "v_exp_f32 v43, v43\n" \
  "v_exp_f32 v44, v44\n" \
  "v_exp_f32 v45, v45\n" \
  "v_cvt_pk_bf16_f32 v111, v42, v43\n" \
  "v_mfma_f32_32x32x16_bf16 v[16:31], v[98:101], v[106:109], v[16:31]\n" \
  "v_exp_f32 v46, v46\n" \
  "v_exp_f32 v47, v47\n" \
  "v_cvt_pk_bf16_f32 v112, v44, v45\n" \
  "v_cvt_pk_bf16_f32 v113, v46, v47\n" \
  "v_exp_f32 v48, v48\n" \
  "v_exp_f32 v49, v49\n" \
  "v_mfma_f32_32x32x16_bf16 v[16:31], v[102:105], v[110:113], v[16:31]\n" \
  "s_mov_b64 exec, s[86:87]\n" \
  "ds_read_b128 v[98:101], v96 offset:32256\n" \
  "ds_read_b128 v[102:105], v97 offset:32256\n" \
  "ds_read_b32 v120, v121\n" \
  "s_mov_b64 exec, -1\n" \
  "La7_%=:\n" \
  "v_exp_f32 v50, v50\n" \
  "v_cvt_pk_bf16_f32 v106, v48, v49\n" \
  "v_exp_f32 v51, v51\n" \
  "v_exp_f32 v52, v52\n" \
  "v_cvt_pk_bf16_f32 v107, v50, v51\n" \
  "v_exp_f32 v53, v53\n" \
  "v_mfma_f32_32x32x8_bf16 v[32:47], v[118:119], v[90:91], 0\n" \
  "v_exp_f32 v54, v54\n" \
  "v_cvt_pk_bf16_f32 v108, v52, v53\n" \
  "v_exp_f32 v55, v55\n" \
  "v_exp_f32 v56, v56\n" \
  "v_cvt_pk_bf16_f32 v109, v54, v55\n" \
  "v_exp_f32 v57, v57\n" \
  "v_mfma_f32_32x32x16_bf16 v[32:47], v[92:95], v[84:87], v[32:47]\n" \
  "s_waitcnt lgkmcnt(0)\n" \
  "v_readfirstlane_b32 s66, v120\n" \
  "s_bitcmp1_b32 s66, s67\n" \
  "s_cbranch_scc0 Lalt3_%=\n" \
  "s_mov_b32 exec_hi, 0\n" \
  "ds_read2st64_b64 v[116:119], v115 offset0:9 offset1:0\n" \
  "s_mov_b32 exec_hi, -1\n" \
  "ds_read_b128 v[92:95], v114 offset:4608\n" \
  "v_exp_f32 v58, v58\n" \
  "v_cvt_pk_bf16_f32 v110, v56, v57\n" \
  "v_exp_f32 v59, v59\n" \
  "v_exp_f32 v60, v60\n" \
  "v_exp_f32 v61, v61\n" \
  "v_cvt_pk_bf16_f32 v111, v58, v59\n" \
  "v_mfma_f32_32x32x16_bf16 v[0:15], v[98:101], v[106:109], v[0:15]\n" \
  "v_exp_f32 v62, v62\n" \
  "v_exp_f32 v63, v63\n" \
  "v_cvt_pk_bf16_f32 v112, v60, v61\n" \
  "v_cvt_pk_bf16_f32 v113, v62, v63\n" \
  "v_exp_f32 v32, v32\n" \
  "v_exp_f32 v33, v33\n" \
  "v_mfma_f32_32x32x16_bf16 v[0:15], v[102:105], v[110:113], v[0:15]\n" \
  "s_waitcnt lgkmcnt(0)\n" \
  "v_exp_f32 v34, v34\n" \
  "v_cvt_pk_bf16_f32 v106, v32, v33\n" \
  "v_exp_f32 v35, v35\n" \
  "v_exp_f32 v36, v36\n" \
  "v_cvt_pk_bf16_f32 v107, v34, v35\n" \
  "v_exp_f32 v37, v37\n" \
  "v_mfma_f32_32x32x8_bf16 v[48:63], v[116:117], v[88:89], v[64:79]\n" \
  "v_exp_f32 v38, v38\n" \
  "v_cvt_pk_bf16_f32 v108, v36, v37\n" \
  "v_exp_f32 v39, v39\n" \
  "v_exp_f32 v40, v40\n" \
  "v_cvt_pk_bf16_f32 v109, v38, v39\n" \
  "v_mfma_f32_32x32x16_bf16 v[48:63], v[92:95], v[80:83], v[48:63]\n" \
  "v_exp_f32 v41, v41\n" \
  "v_exp_f32 v42, v42\n" \
  "v_cvt_pk_bf16_f32 v110, v40, v41\n" \
  "v_exp_f32 v43, v43\n" \
  "v_exp_f32 v44, v44\n" \
  "v_exp_f32 v45, v45\n" \
  "v_cvt_pk_bf16_f32 v111, v42, v43\n" \
  "v_mfma_f32_32x32x16_bf16 v[16:31], v[98:101], v[106:109], v[16:31]\n" \
  "v_exp_f32 v46, v46\n" \
  "v_exp_f32 v47, v47\n" \
  "v_cvt_pk_bf16_f32 v112, v44, v45\n" \
  "v_cvt_pk_bf16_f32 v113, v46, v47\n" \
  "v_exp_f32 v48, v48\n" \
  "v_exp_f32 v49, v49\n" \
  "v_mfma_f32_32x32x16_bf16 v[16:31], v[102:105], v[110:113], v[16:31]\n" \
  "s_mov_b64 exec, s[86:87]\n" \
  "ds_read_b128 v[98:101], v96 offset:36864\n" \
  "ds_read_b128 v[102:105], v97 offset:36864\n" \
  "s_mov_b64 exec, -1\n" \
  "La8_%=:\n" \
  "v_exp_f32 v50, v50\n" \
  "v_cvt_pk_bf16_f32 v106, v48, v49\n" \
  "v_exp_f32 v51, v51\n" \
  "v_exp_f32 v52, v52\n" \
  "v_cvt_pk_bf16_f32 v107, v50, v51\n" \
  "v_exp_f32 v53, v53\n" \
  "v_mfma_f32_32x32x8_bf16 v[32:47], v[116:117], v[90:91], 0\n" \
  "v_exp_f32 v54, v54\n" \
  "v_cvt_pk_bf16_f32 v108, v52, v53\n" \
  "v_exp_f32 v55, v55\n" \
  "v_exp_f32 v56, v56\n" \
  "v_cvt_pk_bf16_f32 v109, v54, v55\n" \
  "v_exp_f32 v57, v57\n" \
  "v_mfma_f32_32x32x16_bf16 v[32:47], v[92:95], v[84:87], v[32:47]\n" \
  "s_waitcnt lgkmcnt(0)\n" \
  "ds_read_b128 v[92:95], v114\n" \
  "v_exp_f32 v58, v58\n" \
  "v_cvt_pk_bf16_f32 v110, v56, v57\n" \
  "v_exp_f32 v59, v59\n" \
  "v_exp_f32 v60, v60\n" \
  "v_exp_f32 v61, v61\n" \
  "v_cvt_pk_bf16_f32 v111, v58, v59\n" \
  "v_mfma_f32_32x32x16_bf16 v[0:15], v[98:101], v[106:109], v[0:15]\n" \
  "v_exp_f32 v62, v62\n" \
  "v_exp_f32 v63, v63\n" \
  "v_cvt_pk_bf16_f32 v112, v60, v61\n" \
  "v_cvt_pk_bf16_f32 v113, v62, v63\n" \
  "v_exp_f32 v32, v32\n" \
  "v_exp_f32 v33, v33\n" \
  "v_mfma_f32_32x32x16_bf16 v[0:15], v[102:105], v[110:113], v[0:15]\n" \
  "s_waitcnt lgkmcnt(0)\n" \
  "v_exp_f32 v34, v34\n" \
  "v_cvt_pk_bf16_f32 v106, v32, v33\n" \
  "v_exp_f32 v35, v35\n" \
  "v_exp_f32 v36, v36\n" \
  "v_cvt_pk_bf16_f32 v107, v34, v35\n" \
  "v_exp_f32 v37, v37\n" \
  "v_mfma_f32_32x32x8_bf16 v[48:63], v[118:119], v[90:91], v[64:79]\n" \
  "v_exp_f32 v38, v38\n" \
  "v_cvt_pk_bf16_f32 v108, v36, v37\n" \
  "v_exp_f32 v39, v39\n" \
  "v_exp_f32 v40, v40\n" \
  "v_cvt_pk_bf16_f32 v109, v38, v39\n" \
  "v_mfma_f32_32x32x16_bf16 v[48:63], v[92:95], v[84:87], v[48:63]\n" \
  "v_exp_f32 v41, v41\n" \
  "v_exp_f32 v42, v42\n" \
  "v_cvt_pk_bf16_f32 v110, v40, v41\n" \
  "v_exp_f32 v43, v43\n" \
  "v_exp_f32 v44, v44\n" \
  "v_exp_f32 v45, v45\n" \
  "v_cvt_pk_bf16_f32 v111, v42, v43\n" \
  "v_mfma_f32_32x32x16_bf16 v[16:31], v[98:101], v[106:109], v[16:31]\n" \
  "v_exp_f32 v46, v46\n" \
  "v_exp_f32 v47, v47\n" \
  "v_cvt_pk_bf16_f32 v112, v44, v45\n" \
  "v_cvt_pk_bf16_f32 v113, v46, v47\n" \
  "v_exp_f32 v48, v48\n" \
  "v_exp_f32 v49, v49\n" \
  "v_mfma_f32_32x32x16_bf16 v[16:31], v[102:105], v[110:113], v[16:31]\n" \
  "s_mov_b64 exec, s[86:87]\n" \
  "ds_read_b128 v[98:101], v96 offset:41472\n" \
  "ds_read_b128 v[102:105], v97 offset:41472\n" \
  "s_mov_b64 exec, -1\n" \
  "v_rcp_f32 v114, v12\n" \
  "s_bitcmp1_b32 s4, 0\n" \
  "s_cbranch_scc0 Lzf60_%=\n" \
  "v_mov_b64 v[8:9], v[10:11]\n" \
  "Lzf60_%=:\n" \
  "v_pk_mul_f32 v[0:1], v[0:1], v[114:115] op_sel_hi:[1,0]\n" \
  "v_pk_mul_f32 v[2:3], v[2:3], v[114:115] op_sel_hi:[1,0]\n" \
  "v_pk_mul_f32 v[4:5], v[4:5], v[114:115] op_sel_hi:[1,0]\n" \
  "v_pk_mul_f32 v[6:7], v[6:7], v[114:115] op_sel_hi:[1,0]\n" \
  "v_cvt_pk_bf16_f32 v0, v0, v1\n" \
  "v_cvt_pk_bf16_f32 v1, v2, v3\n" \
  "v_cvt_pk_bf16_f32 v2, v4, v5\n" \
  "v_cvt_pk_bf16_f32 v3, v6, v7\n" \
  "global_store_dwordx4 v125, v[0:3], s[68:69] sc1\n" \
  "s_mov_b32 exec_hi, 0\n" \
  "v_pk_mul_f32 v[8:9], v[8:9], v[114:115] op_sel_hi:[1,0]\n" \
  "v_cvt_pk_bf16_f32 v8, v8, v9\n" \
  "global_store_dword v126, v8, s[68:69] sc1\n" \
  "s_mov_b32 exec_hi, -1\n" \
  "v_exp_f32 v50, v50\n" \
  "v_cvt_pk_bf16_f32 v106, v48, v49\n" \
  "v_exp_f32 v51, v51\n" \
  "v_exp_f32 v52, v52\n" \
  "v_cvt_pk_bf16_f32 v107, v50, v51\n" \
  "v_exp_f32 v53, v53\n" \
  "v_exp_f32 v54, v54\n" \
  "v_cvt_pk_bf16_f32 v108, v52, v53\n" \
  "v_exp_f32 v55, v55\n" \
  "v_exp_f32 v56, v56\n" \
  "v_cvt_pk_bf16_f32 v109, v54, v55\n" \
  "v_exp_f32 v57, v57\n" \
  "v_exp_f32 v58, v58\n" \
  "v_cvt_pk_bf16_f32 v110, v56, v57\n" \
  "v_exp_f32 v59, v59\n" \
  "v_exp_f32 v60, v60\n" \
  "v_exp_f32 v61, v61\n" \
  "v_cvt_pk_bf16_f32 v111, v58, v59\n" \
  "s_waitcnt lgkmcnt(0)\n" \
  "v_mfma_f32_32x32x16_bf16 v[16:31], v[98:101], v[106:109], v[16:31]\n" \
  "v_exp_f32 v62, v62\n" \
  "v_exp_f32 v63, v63\n" \
  "v_cvt_pk_bf16_f32 v112, v60, v61\n" \
  "v_cvt_pk_bf16_f32 v113, v62, v63\n" \
  "s_nop 1\n" \
  "v_mfma_f32_32x32x16_bf16 v[16:31], v[102:105], v[110:113], v[16:31]\n" \
  "s_branch Ldone_%=\n" \
  "Lt4_%=:\n" \
  "s_sleep 2\n" \
  "s_swappc_b64 s[70:71], s[70:71]\n" \
  "s_branch Lt5_%=\n" \
    \
  "Lalt0_%=:\n" \
  "v_exp_f32 v58, v58\n" \
  "v_cvt_pk_bf16_f32 v110, v56, v57\n" \
  "v_exp_f32 v59, v59\n" \
  "v_exp_f32 v60, v60\n" \
  "v_exp_f32 v61, v61\n" \
  "v_cvt_pk_bf16_f32 v111, v58, v59\n" \
  "v_mfma_f32_32x32x16_bf16 v[0:15], v[98:101], v[106:109], v[0:15]\n" \
  "v_exp_f32 v62, v62\n" \
  "v_exp_f32 v63, v63\n" \
  "v_cvt_pk_bf16_f32 v112, v60, v61\n" \
  "v_cvt_pk_bf16_f32 v113, v62, v63\n" \
  "v_exp_f32 v32, v32\n" \
  "v_exp_f32 v33, v33\n" \
  "v_mfma_f32_32x32x16_bf16 v[0:15], v[102:105], v[110:113], v[0:15]\n" \
  "v_exp_f32 v34, v34\n" \
  "v_cvt_pk_bf16_f32 v106, v32, v33\n" \
  "v_exp_f32 v35, v35\n" \
  "v_exp_f32 v36, v36\n" \
  "v_cvt_pk_bf16_f32 v107, v34, v35\n" \
  "v_exp_f32 v37, v37\n" \
  "v_exp_f32 v38, v38\n" \
  "v_cvt_pk_bf16_f32 v108, v36, v37\n" \
  "v_exp_f32 v39, v39\n" \
  "v_exp_f32 v40, v40\n" \
  "v_cvt_pk_bf16_f32 v109, v38, v39\n" \
  "v_exp_f32 v41, v41\n" \
  "v_exp_f32 v42, v42\n" \
  "v_cvt_pk_bf16_f32 v110, v40, v41\n" \
  "v_exp_f32 v43, v43\n" \
  "v_exp_f32 v44, v44\n" \
  "v_exp_f32 v45, v45\n" \
  "v_cvt_pk_bf16_f32 v111, v42, v43\n" \
  "v_mfma_f32_32x32x16_bf16 v[16:31], v[98:101], v[106:109], 0\n" \
  "v_exp_f32 v46, v46\n" \
  "v_exp_f32 v47, v47\n" \
  "v_cvt_pk_bf16_f32 v112, v44, v45\n" \
  "v_cvt_pk_bf16_f32 v113, v46, v47\n" \
  "s_nop 1\n" \
  "v_mfma_f32_32x32x16_bf16 v[16:31], v[102:105], v[110:113], v[16:31]\n" \
  "s_cmp_eq_u32 s61, 2\n" \
  "s_cbranch_scc1 Learly_%=\n" \
  "s_add_u32 s36, s67, 3\n" \
  "s_branch Lzr62_%=\n" \
  "Lzr61_%=:\n" \
  "s_sleep 1\n" \
  "Lzr62_%=:\n" \
  "ds_read_b32 v120, v121\n" \
  "s_waitcnt lgkmcnt(0)\n" \
  "v_readfirstlane_b32 s66, v120\n" \
  "s_bitcmp1_b32 s66, s36\n" \
  "s_cbranch_scc0 Lzr61_%=\n" \
  "s_mov_b32 exec_hi, 0\n" \
  "ds_read2st64_b64 v[116:119], v115 offset0:63 offset1:54\n" \
  "s_mov_b32 exec_hi, -1\n" \
  "ds_read_b128 v[92:95], v114 offset:32256\n" \
  "s_mov_b64 exec, s[86:87]\n" \
  "ds_read_b128 v[98:101], v96 offset:9216\n" \
  "ds_read_b128 v[102:105], v97 offset:9216\n" \
  "s_mov_b64 exec, -1\n" \
  "s_waitcnt lgkmcnt(3)\n" \
  "v_mfma_f32_32x32x8_bf16 v[48:63], v[116:117], v[88:89], 0\n" \
  "s_waitcnt lgkmcnt(2)\n" \
  "v_mfma_f32_32x32x16_bf16 v[48:63], v[92:95], v[80:83], v[48:63]\n" \
  "s_nop 11\n" \
  "v_exp_f32 v48, v48\n" \
  "v_exp_f32 v49, v49\n" \
  "s_branch La2_%=\n" \
  "Lhf2_%=:\n" \
  "s_branch Lzw64_%=\n" \
  "Lzw63_%=:\n" \
  "s_sleep 1\n" \
  "Lzw64_%=:\n" \
  "ds_read_b32 v120, v121\n" \
  "s_waitcnt lgkmcnt(0)\n" \
  "v_readfirstlane_b32 s66, v120\n" \
  "s_bitcmp1_b32 s66, s67\n" \
  "s_cbranch_scc0 Lzw63_%=\n" \
  "s_branch Lhg2_%=\n" \
  "Lt5_%=:\n" \
  "s_sleep 2\n" \
  "s_swappc_b64 s[70:71], s[70:71]\n" \
  "s_branch Lt6_%=\n" \
  "Lalt1_%=:\n" \
  "v_exp_f32 v58, v58\n" \
  "v_cvt_pk_bf16_f32 v110, v56, v57\n" \
  "v_exp_f32 v59, v59\n" \
  "v_exp_f32 v60, v60\n" \
  "v_exp_f32 v61, v61\n" \
  "v_cvt_pk_bf16_f32 v111, v58, v59\n" \
  "v_mfma_f32_32x32x16_bf16 v[0:15], v[98:101], v[106:109], v[0:15]\n" \
  "v_exp_f32 v62, v62\n" \
  "v_exp_f32 v63, v63\n" \
  "v_cvt_pk_bf16_f32 v112, v60, v61\n" \
  "v_cvt_pk_bf16_f32 v113, v62, v63\n" \
  "v_exp_f32 v32, v32\n" \
  "v_exp_f32 v33, v33\n" \
  "v_mfma_f32_32x32x16_bf16 v[0:15], v[102:105], v[110:113], v[0:15]\n" \
  "v_exp_f32 v34, v34\n" \
  "v_cvt_pk_bf16_f32 v106, v32, v33\n" \
  "v_exp_f32 v35, v35\n" \
  "v_exp_f32 v36, v36\n" \
  "v_cvt_pk_bf16_f32 v107, v34, v35\n" \
  "v_exp_f32 v37, v37\n" \
  "v_exp_f32 v38, v38\n" \
  "v_cvt_pk_bf16_f32 v108, v36, v37\n" \
  "v_exp_f32 v39, v39\n" \
  "v_exp_f32 v40, v40\n" \
  "v_cvt_pk_bf16_f32 v109, v38, v39\n" \
  "v_exp_f32 v41, v41\n" \
  "v_exp_f32 v42, v42\n" \
  "v_cvt_pk_bf16_f32 v110, v40, v41\n" \
  "v_exp_f32 v43, v43\n" \
  "v_exp_f32 v44, v44\n" \
  "v_exp_f32 v45, v45\n" \
  "v_cvt_pk_bf16_f32 v111, v42, v43\n" \
  "v_mfma_f32_32x32x16_bf16 v[16:31], v[98:101], v[106:109], v[16:31]\n" \
  "v_exp_f32 v46, v46\n" \
  "v_exp_f32 v47, v47\n" \
  "v_cvt_pk_bf16_f32 v112, v44, v45\n" \
  "v_cvt_pk_bf16_f32 v113, v46, v47\n" \
  "s_nop 1\n" \
  "v_mfma_f32_32x32x16_bf16 v[16:31], v[102:105], v[110:113], v[16:31]\n" \
  "s_cmp_eq_u32 s61, 4\n" \
  "s_cbranch_scc1 Learly_%=\n" \
  "s_add_u32 s36, s67, 3\n" \
  "s_branch Lzr66_%=\n" \
  "Lzr65_%=:\n" \
  "s_sleep 1\n" \
  "Lzr66_%=:\n" \
  "ds_read_b32 v120, v121\n" \
  "s_waitcnt lgkmcnt(0)\n" \
  "v_readfirstlane_b32 s66, v120\n" \
  "s_bitcmp1_b32 s66, s36\n" \
  "s_cbranch_scc0 Lzr65_%=\n" \
  "s_mov_b32 exec_hi, 0\n" \
  "ds_read2st64_b64 v[116:119], v115 offset0:45 offset1:36\n" \
  "s_mov_b32 exec_hi, -1\n" \
  "ds_read_b128 v[92:95], v114 offset:23040\n" \
  "s_mov_b64 exec, s[86:87]\n" \
  "ds_read_b128 v[98:101], v96 offset:18432\n" \
  "ds_read_b128 v[102:105], v97 offset:18432\n" \
  "s_mov_b64 exec, -1\n" \
  "s_waitcnt lgkmcnt(3)\n" \
  "v_mfma_f32_32x32x8_bf16 v[48:63], v[116:117], v[88:89], 0\n" \
  "s_waitcnt lgkmcnt(2)\n" \
  "v_mfma_f32_32x32x16_bf16 v[48:63], v[92:95], v[80:83], v[48:63]\n" \
  "s_nop 11\n" \
  "v_exp_f32 v48, v48\n" \
  "v_exp_f32 v49, v49\n" \
  "s_branch La4_%=\n" \
  "Lhf4_%=:\n" \
  "s_branch Lzw68_%=\n" \
  "Lzw67_%=:\n" \
  "s_sleep 1\n" \
  "Lzw68_%=:\n" \
  "ds_read_b32 v120, v121\n" \
  "s_waitcnt lgkmcnt(0)\n" \
  "v_readfirstlane_b32 s66, v120\n" \
  "s_bitcmp1_b32 s66, s67\n" \
  "s_cbranch_scc0 Lzw67_%=\n" \
  "s_branch Lhg4_%=\n" \
  "Lalt2_%=:\n" \
  "s_getpc_b64 s[70:71]\n" \
  "s_branch Lrest_%=\n" \
  "s_cmp_eq_u32 s61, 6\n" \
  "s_cbranch_scc1 Learly_%=\n" \
  "s_add_u32 s36, s67, 0\n" \
  "s_branch Lzr70_%=\n" \
  "Lzr69_%=:\n" \
  "s_sleep 1\n" \
  "Lzr70_%=:\n" \
  "ds_read_b32 v120, v121\n" \
  "s_waitcnt lgkmcnt(0)\n" \
  "v_readfirstlane_b32 s66, v120\n" \
  "s_bitcmp1_b32 s66, s36\n" \
  "s_cbranch_scc0 Lzr69_%=\n" \
  "s_mov_b32 exec_hi, 0\n" \
  "ds_read2st64_b64 v[116:119], v115 offset0:27 offset1:18\n" \
  "s_mov_b32 exec_hi, -1\n" \
  "ds_read_b128 v[92:95], v114 offset:13824\n" \
  "s_mov_b64 exec, s[86:87]\n" \
  "ds_read_b128 v[98:101], v96 offset:27648\n" \
  "ds_read_b128 v[102:105], v97 offset:27648\n" \
  "s_mov_b64 exec, -1\n" \
  "s_waitcnt lgkmcnt(3)\n" \
  "v_mfma_f32_32x32x8_bf16 v[48:63], v[116:117], v[88:89], 0\n" \
  "s_waitcnt lgkmcnt(2)\n" \
  "v_mfma_f32_32x32x16_bf16 v[48:63], v[92:95], v[80:83], v[48:63]\n" \
  "s_nop 11\n" \
  "v_exp_f32 v48, v48\n" \
  "v_exp_f32 v49, v49\n" \
  "s_branch La6_%=\n" \
  "Lalt3_%=:\n" \
  "s_getpc_b64 s[70:71]\n" \
  "s_branch Lrest_%=\n" \
  "s_cmp_eq_u32 s61, 8\n" \
  "s_cbranch_scc1 Learly_%=\n" \
  "s_add_u32 s36, s67, 0\n" \
  "s_branch Lzr72_%=\n" \
  "Lzr71_%=:\n" \
  "s_sleep 1\n" \
  "Lzr72_%=:\n" \
  "ds_read_b32 v120, v121\n" \
  "s_waitcnt lgkmcnt(0)\n" \
  "v_readfirstlane_b32 s66, v120\n" \
  "s_bitcmp1_b32 s66, s36\n" \
  "s_cbranch_scc0 Lzr71_%=\n" \
  "s_mov_b32 exec_hi, 0\n" \
  "ds_read2st64_b64 v[116:119], v115 offset0:9 offset1:0\n" \
  "s_mov_b32 exec_hi, -1\n" \
  "ds_read_b128 v[92:95], v114 offset:4608\n" \
  "s_mov_b64 exec, s[86:87]\n" \
  "ds_read_b128 v[98:101], v96 offset:36864\n" \
  "ds_read_b128 v[102:105], v97 offset:36864\n" \
  "s_mov_b64 exec, -1\n" \
  "s_waitcnt lgkmcnt(3)\n" \
  "v_mfma_f32_32x32x8_bf16 v[48:63], v[116:117], v[88:89], v[64:79]\n" \
  "s_waitcnt lgkmcnt(2)\n" \
  "v_mfma_f32_32x32x16_bf16 v[48:63], v[92:95], v[80:83], v[48:63]\n" \
  "s_nop 11\n" \
  "v_exp_f32 v48, v48\n" \
  "v_exp_f32 v49, v49\n" \
  "s_branch La8_%=\n" \
  "Lrest_%=:\n" \
  "v_exp_f32 v58, v58\n" \
  "v_cvt_pk_bf16_f32 v110, v56, v57\n" \
  "v_exp_f32 v59, v59\n" \
  "v_exp_f32 v60, v60\n" \
  "v_exp_f32 v61, v61\n" \
  "v_cvt_pk_bf16_f32 v111, v58, v59\n" \
  "v_mfma_f32_32x32x16_bf16 v[0:15], v[98:101], v[106:109], v[0:15]\n" \
  "v_exp_f32 v62, v62\n" \
  "v_exp_f32 v63, v63\n" \
  "v_cvt_pk_bf16_f32 v112, v60, v61\n" \
  "v_cvt_pk_bf16_f32 v113, v62, v63\n" \
  "v_exp_f32 v32, v32\n" \
  "v_exp_f32 v33, v33\n" \
  "v_mfma_f32_32x32x16_bf16 v[0:15], v[102:105], v[110:113], v[0:15]\n" \
  "v_exp_f32 v34, v34\n" \
  "v_cvt_pk_bf16_f32 v106, v32, v33\n" \
  "v_exp_f32 v35, v35\n" \
  "v_exp_f32 v36, v36\n" \
  "v_cvt_pk_bf16_f32 v107, v34, v35\n" \
  "v_exp_f32 v37, v37\n" \
  "v_exp_f32 v38, v38\n" \
  "v_cvt_pk_bf16_f32 v108, v36, v37\n" \
  "v_exp_f32 v39, v39\n" \
  "v_exp_f32 v40, v40\n" \
  "v_cvt_pk_bf16_f32 v109, v38, v39\n" \
  "v_exp_f32 v41, v41\n" \
  "v_exp_f32 v42, v42\n" \
  "v_cvt_pk_bf16_f32 v110, v40, v41\n" \
  "v_exp_f32 v43, v43\n" \
  "v_exp_f32 v44, v44\n" \
  "v_exp_f32 v45, v45\n" \
  "v_cvt_pk_bf16_f32 v111, v42, v43\n" \
  "v_mfma_f32_32x32x16_bf16 v[16:31], v[98:101], v[106:109], v[16:31]\n" \
  "v_exp_f32 v46, v46\n" \
  "v_exp_f32 v47, v47\n" \
  "v_cvt_pk_bf16_f32 v112, v44, v45\n" \
  "v_cvt_pk_bf16_f32 v113, v46, v47\n" \
  "s_nop 1\n" \
  "v_mfma_f32_32x32x16_bf16 v[16:31], v[102:105], v[110:113], v[16:31]\n" \
  "s_add_u32 s70, s70, 4\n" \
  "s_addc_u32 s71, s71, 0\n" \
  "s_setpc_b64 s[70:71]\n" \
  "Lt6_%=:\n" \
  "s_sleep 2\n" \
  "s_swappc_b64 s[70:71], s[70:71]\n" \
  "s_branch Lt7_%=\n" \
  "Lt7_%=:\n" \
  "s_swappc_b64 s[70:71], s[70:71]\n" \
  "s_branch Lt7_%=\n" \
  "Learly_%=:\n" \
  "v_rcp_f32 v114, v12\n" \
  "s_bitcmp1_b32 s4, 0\n" \
  "s_cbranch_scc0 Lzf73_%=\n" \
  "v_mov_b64 v[8:9], v[10:11]\n" \
  "Lzf73_%=:\n" \
  "v_pk_mul_f32 v[0:1], v[0:1], v[114:115] op_sel_hi:[1,0]\n" \
  "v_pk_mul_f32 v[2:3], v[2:3], v[114:115] op_sel_hi:[1,0]\n" \
  "v_pk_mul_f32 v[4:5], v[4:5], v[114:115] op_sel_hi:[1,0]\n" \
  "v_pk_mul_f32 v[6:7], v[6:7], v[114:115] op_sel_hi:[1,0]\n" \
  "v_cvt_pk_bf16_f32 v0, v0, v1\n" \
  "v_cvt_pk_bf16_f32 v1, v2, v3\n" \
  "v_cvt_pk_bf16_f32 v2, v4, v5\n" \
  "v_cvt_pk_bf16_f32 v3, v6, v7\n" \
  "global_store_dwordx4 v125, v[0:3], s[68:69] sc1\n" \
  "s_mov_b32 exec_hi, 0\n" \
  "v_pk_mul_f32 v[8:9], v[8:9], v[114:115] op_sel_hi:[1,0]\n" \
  "v_cvt_pk_bf16_f32 v8, v8, v9\n" \
  "global_store_dword v126, v8, s[68:69] sc1\n" \
  "s_mov_b32 exec_hi, -1\n" \
    \
  "Ldone_%=:\n" \
  "s_lshl_b32 s38, s7, 3\n" \
  "s_add_u32 s38, s38, 158720\n" \
  "v_mov_b32 v36, s38\n" \
  "ds_read_b64 v[38:39], v36\n" \
  "v_mov_b32 v36, 158848\n" \
  "v_subrev_u32 v32, 813694976, v12\n" \
  "s_mul_i32 s38, s7, 528\n" \
  "s_lshl_b32 s39, s10, 4\n" \
  "s_add_u32 s38, s38, s39\n" \
  "s_add_u32 s38, s38, s6\n" \
  "s_lshl_b32 s38, s38, 3\n" \
  "v_lshlrev_b32 v33, 9, v124\n" \
  "v_add_u32 v33, s38, v33\n" \
  "s_lshl_b32 s38, s10, 2\n" \
  "v_rcp_f32 v34, v28\n" \
  "s_bitcmp1_b32 s4, 0\n" \
  "s_cbranch_scc0 Lzf74_%=\n" \
  "v_mov_b64 v[24:25], v[26:27]\n" \
  "Lzf74_%=:\n" \
  "v_pk_mul_f32 v[16:17], v[16:17], v[34:35] op_sel_hi:[1,0]\n" \
  "v_pk_mul_f32 v[18:19], v[18:19], v[34:35] op_sel_hi:[1,0]\n" \
  "v_pk_mul_f32 v[20:21], v[20:21], v[34:35] op_sel_hi:[1,0]\n" \
  "v_pk_mul_f32 v[22:23], v[22:23], v[34:35] op_sel_hi:[1,0]\n" \
  "v_cvt_pk_bf16_f32 v16, v16, v17\n" \
  "v_cvt_pk_bf16_f32 v17, v18, v19\n" \
  "v_cvt_pk_bf16_f32 v18, v20, v21\n" \
  "v_cvt_pk_bf16_f32 v19, v22, v23\n" \
  "global_store_dwordx4 v125, v[16:19], s[28:29] sc1\n" \
  "s_mov_b32 exec_hi, 0\n" \
  "v_pk_mul_f32 v[24:25], v[24:25], v[34:35] op_sel_hi:[1,0]\n" \
  "v_cvt_pk_bf16_f32 v24, v24, v25\n" \
  "global_store_dword v126, v24, s[28:29] sc1\n" \
  "s_mov_b32 exec_hi, -1\n" \
  "s_mov_b64 exec, 1\n" \
  "ds_add_u32 v36, v122 offset:56\n" \
  "s_mov_b64 exec, -1\n" \
  "v_subrev_u32 v34, 813694976, v28\n" \
  "v_max_u32 v32, v32, v34\n" \
  "v_add_u32 v36, s38, v36\n" \
  "v_cmp_lt_u32 vcc, 503316480, v32\n" \
  "s_waitcnt lgkmcnt(1)\n" \
  "s_or_b32 s11, vcc_lo, vcc_hi\n" \
  "Lend_%=:\n"
#define MAIN_CLOB "v1","v2","v3","v4","v5","v6","v7","v8","v9","v10","v11","v12","v13","v14","v15","v16","v17","v18","v19","v20","v21","v22","v23","v24","v25","v26","v27","v28","v29","v30","v31","v32","v34","v35","v37","v40","v41","v42","v43","v44","v45","v46","v47","v48","v49","v50","v51","v52","v53","v54","v55","v56","v57","v58","v59","v60","v61","v62","v63","v64","v65","v66","v67","v68","v69","v70","v71","v72","v73","v74","v75","v76","v77","v78","v79","v92","v93","v94","v95","v96","v97","v98","v99","v100","v101","v102","v103","v104","v105","v106","v107","v108","v109","v110","v111","v112","v113","v114","v115","v116","v117","v118","v119","v120","v121","v122","v123","s3","s5","s8","s9","s12","s13","s14","s15","s16","s17","s18","s19","s20","s21","s22","s23","s30","s31","s36","s37","s38","s39","s40","s41","s42","s43","s44","s45","s46","s47","s48","s49","s50","s51","s52","s53","s54","s55","s56","s57","s58","s59","s60","s61","s62","s63","s64","s65","s66","s67","s68","s69","s70","s71","s72","s73","s74","s75","s76","s77","s78","s79","s80","s81","s82","s83","s84","s85","s86","s87","s88","s89","s90","s91","s92","s93"
#define REDO_ASM \
  "s_waitcnt vmcnt(0)\n" \
  "s_add_u32 s36, s28, 36864\n" \
  "s_addc_u32 s37, s29, 0\n" \
  "v_rcp_f32 v34, v12\n" \
  "s_bitcmp1_b32 s4, 0\n" \
  "s_cbranch_scc0 Lzf75_%=\n" \
  "v_mov_b64 v[8:9], v[10:11]\n" \
  "Lzf75_%=:\n" \
  "v_pk_mul_f32 v[0:1], v[0:1], v[34:35] op_sel_hi:[1,0]\n" \
  "v_pk_mul_f32 v[2:3], v[2:3], v[34:35] op_sel_hi:[1,0]\n" \
  "v_pk_mul_f32 v[4:5], v[4:5], v[34:35] op_sel_hi:[1,0]\n" \
  "v_pk_mul_f32 v[6:7], v[6:7], v[34:35] op_sel_hi:[1,0]\n" \
  "v_cvt_pk_bf16_f32 v0, v0, v1\n" \
  "v_cvt_pk_bf16_f32 v1, v2, v3\n" \
  "v_cvt_pk_bf16_f32 v2, v4, v5\n" \
  "v_cvt_pk_bf16_f32 v3, v6, v7\n" \
  "global_store_dwordx4 v125, v[0:3], s[36:37] sc1\n" \
  "s_mov_b32 exec_hi, 0\n" \
  "v_pk_mul_f32 v[8:9], v[8:9], v[34:35] op_sel_hi:[1,0]\n" \
  "v_cvt_pk_bf16_f32 v8, v8, v9\n" \
  "global_store_dword v126, v8, s[36:37] sc1\n" \
  "s_mov_b32 exec_hi, -1\n" \
  "v_rcp_f32 v34, v28\n" \
  "s_bitcmp1_b32 s4, 0\n" \
  "s_cbranch_scc0 Lzf76_%=\n" \
  "v_mov_b64 v[24:25], v[26:27]\n" \
  "Lzf76_%=:\n" \
  "v_pk_mul_f32 v[16:17], v[16:17], v[34:35] op_sel_hi:[1,0]\n" \
  "v_pk_mul_f32 v[18:19], v[18:19], v[34:35] op_sel_hi:[1,0]\n" \
  "v_pk_mul_f32 v[20:21], v[20:21], v[34:35] op_sel_hi:[1,0]\n" \
  "v_pk_mul_f32 v[22:23], v[22:23], v[34:35] op_sel_hi:[1,0]\n" \
  "v_cvt_pk_bf16_f32 v16, v16, v17\n" \
  "v_cvt_pk_bf16_f32 v17, v18, v19\n" \
  "v_cvt_pk_bf16_f32 v18, v20, v21\n" \
  "v_cvt_pk_bf16_f32 v19, v22, v23\n" \
  "global_store_dwordx4 v125, v[16:19], s[28:29] sc1\n" \
  "s_mov_b32 exec_hi, 0\n" \
  "v_pk_mul_f32 v[24:25], v[24:25], v[34:35] op_sel_hi:[1,0]\n" \
  "v_cvt_pk_bf16_f32 v24, v24, v25\n" \
  "global_store_dword v126, v24, s[28:29] sc1\n" \
  "s_mov_b32 exec_hi, -1\n" \
  "s_waitcnt vmcnt(0)\n"
#define TAIL_ASM \
  "v_mov_b32 v37, 158872\n" \
  "s_branch Lzw78_%=\n" \
  "Lzw77_%=:\n" \
  "s_sleep 1\n" \
  "Lzw78_%=:\n" \
  "ds_read_b32 v35, v37\n" \
  "s_waitcnt lgkmcnt(0)\n" \
  "v_readfirstlane_b32 s39, v35\n" \
  "s_bitcmp1_b32 s39, 0\n" \
  "s_cbranch_scc0 Lzw77_%=\n" \
  "v_mov_b32 v37, 1\n" \
  "s_waitcnt vmcnt(0)\n" \
  "s_mov_b64 exec, 1\n" \
  "ds_add_rtn_u32 v35, v36, v37 offset:44\n" \
  "s_mov_b64 exec, -1\n" \
  "s_waitcnt lgkmcnt(0)\n" \
  "v_readfirstlane_b32 s39, v35\n" \
  "s_cmp_lg_u32 s39, 3\n" \
  "s_cbranch_scc1 Lf1_%=\n" \
  "s_mov_b64 exec, 0xff\n" \
  "global_atomic_swap_x2 v33, v[38:39], s[24:25] offset:256 sc1\n" \
  "s_mov_b64 exec, -1\n" \
  "Lf1_%=:\n"

DI u32 pk(float a,float b){ f32x2 v={a,b}; return __builtin_bit_cast(u32,__builtin_convertvector(v,bf16x2)); }
DI bf16x8 cat(s16x4 a,s16x4 b){ return __builtin_bit_cast(bf16x8,__builtin_shufflevector(a,b,0,1,2,3,4,5,6,7)); }
DI void lds_wait(u32 a,u32 bit){ u32 o,t;
  asm volatile("s_branch Lw1_%=\nLw0_%=:\n\ts_sleep 1\nLw1_%=:\n\tds_read_b32 %1, %2\n\ts_waitcnt lgkmcnt(0)\n\tv_readfirstlane_b32 %0, %1\n\ts_bitcmp1_b32 %0, %3\n\ts_cbranch_scc0 Lw0_%=":"=&s"(o),"=&v"(t):"v"(a),"s"(bit):"memory","scc"); }
DI float rowmax(const f32x16& s){
  float m=s[0];
#pragma unroll
  for(int e=1;e<16;e++) m=fmaxf(m,s[e]);
  float m2=m;
  asm volatile("s_nop 1\n\tv_permlane32_swap_b32 %0, %1":"+v"(m),"+v"(m2));
  return fmaxf(m,m2);
}

__global__ void __launch_bounds__(1024,4) fused(Params p){
  bf16x8 qam,qbm; u32 tid=threadIdx.x,fa,sa,bad; u64 tag; u32x2 ea,eb; int lane,l,g,ch,hi; u32 om,oe; int bid=blockIdx.x;
  GAS char *ctl,*kv,*ow;
  asm volatile(MAIN_ASM
    :"+{v0}"(tid),"={v33}"(fa),"={v36}"(sa),"={v[38:39]}"(tag),"={s11}"(bad),"={v[80:83]}"(qam),"={v[84:87]}"(qbm),"={v[88:89]}"(ea),"={v[90:91]}"(eb),"={v124}"(lane),"={v125}"(om),"={v126}"(oe),
     "+{s2}"(bid),"={s4}"(l),"={s6}"(g),"={s7}"(ch),"={s10}"(hi),"={s[24:25]}"(ctl),"={s[26:27]}"(kv),"={s[28:29]}"(ow)
    :"{s[0:1]}"(__builtin_amdgcn_kernarg_segment_ptr())
    :"memory","scc","vcc",MAIN_CLOB);
  const int r=lane&31, h=lane>>5, ia=2*hi+1;
  if(__builtin_expect(bad!=0,0)){
    const f32x16 zero={0,0,0,0,0,0,0,0,0,0,0,0,0,0,0,0};
    f32x16 oa=zero,ob=zero;
    lds_wait(S_DR,3);
#pragma nounroll
    for(int u=0;u<2;u++){
      const int I=ch*4+ia-u, nt=I<8?I+1:9;
      u32x2 qe=u?eb:ea; if(h) qe[0]=0;
      const bf16x8 qm=u?qbm:qam;
      float m=-3e38f; f32x16 o=zero;
#pragma nounroll
      for(int t=0;t<nt;t++){
        const int T=I-t; const GAS char* im=kv+(size_t)((T>>2)*8+g)*KVS;
        const GAS char* kp=im+(T&3)*TS; const GAS char* vp=im+18432+(3-(T&3))*TS;
        const u64 k0=__hip_atomic_load((const GAS u64*)(kp+l*1024+lane*16),RLX_AGENT), k1=__hip_atomic_load((const GAS u64*)(kp+l*1024+lane*16+8),RLX_AGENT);
        const u64 ke=__hip_atomic_load((const GAS u64*)(kp+4096+r*16+(l>>1)*8),RLX_AGENT);
        f32x16 s=MF8(__builtin_bit_cast(s16x4,ke),__builtin_bit_cast(s16x4,qe),zero);
        s=MF32(cat(__builtin_bit_cast(s16x4,k0),__builtin_bit_cast(s16x4,k1)),qm,s);
        int kh=t==0?r-4*h:99, kl=t==8?r-4*h:-1;
        asm volatile("":"+v"(kh),"+v"(kl));
        u64 vv[4];
#pragma unroll
        for(int j=0;j<4;j++){
          const GAS char* vq=vp+(r<16?l*1024+(j>>1)*512+h*256+r*16:4096+(j>>1)*256+h*128+(l>>1)*64+(r&3)*16)+(j&1)*8;
          vv[j]=__hip_atomic_load((const GAS u64*)vq,RLX_AGENT);
          if(r>=20) vv[j]=0x3f803f803f803f80ull; }
#pragma unroll
        for(int e=0;e<16;e++){ const int k=(e&3)+8*(e>>2); if(k>kh||k<=kl) s[e]=-__builtin_inff(); }
        const float mn=fmaxf(m,rowmax(s)), al=__builtin_amdgcn_exp2f(m-mn); m=mn;
#pragma unroll
        for(int e=0;e<16;e++){ o[e]*=al; s[e]=__builtin_amdgcn_exp2f(s[e]-mn); }
        o=MF32(cat(__builtin_bit_cast(s16x4,vv[0]),__builtin_bit_cast(s16x4,vv[1])),__builtin_bit_cast(bf16x8,u32x4{pk(s[0],s[1]),pk(s[2],s[3]),pk(s[4],s[5]),pk(s[6],s[7])}),o);
        o=MF32(cat(__builtin_bit_cast(s16x4,vv[2]),__builtin_bit_cast(s16x4,vv[3])),__builtin_bit_cast(bf16x8,u32x4{pk(s[8],s[9]),pk(s[10],s[11]),pk(s[12],s[13]),pk(s[14],s[15])}),o);
      }
      if(u) ob=o; else oa=o;
    }
    asm volatile("s_nop 7\n\ts_nop 7\n" REDO_ASM
      :"+{v[0:15]}"(oa),"+{v[16:31]}"(ob):"{v125}"(om),"{v126}"(oe),"{s4}"(l),"{s[28:29]}"(ow):"memory","scc","v34","v35","s36","s37");
  }
  asm volatile(TAIL_ASM
    :
    :"{v33}"(fa),"{v36}"(sa),"{v[38:39]}"(tag),"{s[24:25]}"(ctl)
    :"memory","scc","v35","v37","s39");
}

extern "C" void kernel_launch(void* const* d_in, const int* in_sizes, int n_in, void* d_out, int out_size, void* d_ws, size_t ws_size, hipStream_t stream){
  Params p;
  for(int b=0;b<2;b++){
    char* ws=(char*)d_ws;
    p.b[b].x=d_in[3*b]; p.b[b].w=d_in[3*b+1]; p.b[b].wo=d_in[3*b+2]; p.b[b].out=(char*)d_out+b*1152;
    p.b[b].ctl=ws+W_CTL+b*16*4224; p.b[b].kv=ws+W_KV+(size_t)b*128*KVS; p.b[b].o=ws+W_O+(size_t)b*32*OHS; p.b[b].spare=ws;
  }
  for(int i=0;i<32;i++){
    const int cl=i>>3, g=i&7; int v[9],n=0;
    const int f=(g+cl)%3, ld=(g%7?(g+3*cl)%6:2+cl), pos=ld<4?ld+1:5;
    for(int m=0;m<9;m++){ if(n==pos) v[n++]=3+ld; if(m<3) v[n++]=(f+m)%3; else if(m!=3+ld) v[n++]=m; }
    if(n<9) v[n]=3+ld;
    u64 o=0; for(int m=0;m<9;m++) o|=(u64)v[m]<<(4*m);
    p.order[i]=o|(u64)(6)<<36;
  }
  (void)hipFuncSetAttribute((const void*)fused,hipFuncAttributeMaxDynamicSharedMemorySize,LDS_BYTES);
  fused<<<256,1024,LDS_BYTES,stream>>>(p);
}
```
